```python
import torch

def _softmax(x, axis=-1):
    return torch.softmax(x, dim=axis)
def _gelu(x):
    return torch.nn.functional.gelu(x, approximate="tanh")
def _silu(x):
    return torch.nn.functional.silu(x)
def _sigmoid(x):
    return torch.sigmoid(x)
def _layernorm(x, axis=-1, eps=1e-5):
    m = x.mean(dim=axis, keepdim=True)
    v = x.var(dim=axis, keepdim=True, unbiased=False)
    return (x - m) / torch.sqrt(v + eps)
def _scatter_add(tgt, idx, src, axis=0):
    return tgt.clone().index_add_(axis, idx, src)
def _segment_sum(src, seg, n):
    out = torch.zeros((n,) + src.shape[1:], dtype=src.dtype)
    return out.index_add_(0, seg, src)
def _pad(x, pw):
    flat = tuple(v for pair in reversed(pw) for v in pair)
    return torch.nn.functional.pad(x, flat)

kernel_name = 'concat_heads__ssm_conv_s4d__mha_block__prompt'

def setup_inputs():

    _w = (lambda a: (lambda t: t.to(torch.bfloat16) if t.is_floating_point() else t)(torch.as_tensor(a)))
    def _A_setup():
        Bsz, L, D, N = 1, 2048, 512, 128
        return {
            "u": _w((lambda *s: torch.randn(*s, dtype=torch.bfloat16) * 0.05)(Bsz, L, D)),
            "log_A_real": _w((lambda *s: torch.randn(*s, dtype=torch.bfloat16) * 6.4)(D, N) * 0.5),
            "A_imag": _w((lambda *s: torch.randn(*s, dtype=torch.bfloat16) * 0.1)(D, N) * 3.14),
            "C_real": _w((lambda *s: torch.randn(*s, dtype=torch.bfloat16) * 0.4)(D, N) / N ** 0.5),
            "C_imag": _w((lambda *s: torch.randn(*s, dtype=torch.bfloat16) * 0.4)(D, N) / N ** 0.5),
            "log_dt": _w((lambda *s: torch.randn(*s, dtype=torch.bfloat16) * 0.1)(D) * 0.5 - 4.0),
            "D_skip": _w((lambda *s: torch.randn(*s, dtype=torch.bfloat16) * 0.1)(D)),
        }
    _A_in = _A_setup()

    def _B_setup():
        B, S, D, H = 1, 2048, 512, 32
        return {
            "x": _w((lambda *s: torch.randn(*s, dtype=torch.bfloat16) * 0.2)(B, S, D)),
            "wqkv": _w((lambda *s: torch.randn(*s, dtype=torch.bfloat16) * 0.2)(D, 3 * D)),
            "wo": _w((lambda *s: torch.randn(*s, dtype=torch.bfloat16) * 0.05)(D, D)),
        }
    _B_in = _B_setup()

    return {
        "A_u": _A_in["u"],
        "A_log_A_real": _A_in["log_A_real"],
        "A_A_imag": _A_in["A_imag"],
        "A_C_real": _A_in["C_real"],
        "A_C_imag": _A_in["C_imag"],
        "A_log_dt": _A_in["log_dt"],
        "A_D_skip": _A_in["D_skip"],
        "B_x": _B_in["x"],
        "B_wqkv": _B_in["wqkv"],
        "B_wo": _B_in["wo"],
    }

def reference(A_u, A_log_A_real, A_A_imag, A_C_real, A_C_imag, A_log_dt, A_D_skip, B_x, B_wqkv, B_wo):
    def _A_ref(u, log_A_real, A_imag, C_real, C_imag, log_dt, D_skip):
        L = u.shape[1]
        dt = torch.exp(log_dt)
        A = -torch.exp(log_A_real) + 1j * A_imag
        C = (C_real + 1j * C_imag) * dt[:, None]
        pos = (lambda x, dt: x.to(dt))(torch.arange(L), log_dt.dtype)
        P = torch.exp((A * dt[:, None])[:, :, None] * pos[None, None, :])
        K = 2.0 * torch.einsum("dn,dnl->dl", C, P).real
        L2 = 2 * L
        u_t = (lambda x, *d: x.permute(*d))(u, 0, 2, 1)
        Kf = (lambda x, *a, **k: torch.fft.rfft(x.float(), *a, **k))(K, L2)
        Uf = (lambda x, *a, **k: torch.fft.rfft(x.float(), *a, **k))(u_t, L2)
        y = (lambda x, *a, **k: torch.fft.irfft(x, *a, **k).to(torch.bfloat16))(Uf * Kf[None, :, :], L2)[:, :, :L]
        y = (lambda x, *d: x.permute(*d))(y, 0, 2, 1)
        return y + u * D_skip[None, None, :]

    def _B_ref(x, wqkv, wo):
        B, S, D = x.shape
        H = 32
        Dh = D // H
        qkv = (lambda x, *d: x.permute(*d))((x @ wqkv).reshape(B, S, 3, H, Dh), 2, 0, 3, 1, 4)
        q, k, v = qkv[0], qkv[1], qkv[2]
        scores = torch.einsum("bhqd,bhkd->bhqk", q, k) * (Dh ** -0.5)
        p = _softmax(scores, axis=-1)
        o = torch.einsum("bhqk,bhkd->bhqd", p, v)
        return (lambda x, *d: x.permute(*d))(o, 0, 2, 1, 3).reshape(B, S, D) @ wo

    a = _A_ref(A_u, A_log_A_real, A_A_imag, A_C_real, A_C_imag, A_log_dt, A_D_skip)
    b = _B_ref(B_x, B_wqkv, B_wo)
    return torch.concatenate([a, b], axis=-1)
```

```cpp
#include <hip/hip_runtime.h>
#include <stdint.h>

typedef float f32x16 __attribute__((ext_vector_type(16)));
typedef float f32x4 __attribute__((ext_vector_type(4)));
typedef float f32x2 __attribute__((ext_vector_type(2)));
typedef unsigned u32x4 __attribute__((ext_vector_type(4)));
typedef unsigned u32x2 __attribute__((ext_vector_type(2)));
typedef short s16x4 __attribute__((ext_vector_type(4)));
typedef short s16x8 __attribute__((ext_vector_type(8)));
typedef _Float16 f16x8 __attribute__((ext_vector_type(8)));
typedef _Float16 f16x2 __attribute__((ext_vector_type(2)));
typedef __bf16 bf16x8 __attribute__((ext_vector_type(8)));
typedef __bf16 bf16x2 __attribute__((ext_vector_type(2)));
#define LAS __attribute__((address_space(3)))
#define GAS __attribute__((address_space(1)))
typedef GAS unsigned long long gu64;

#define ATT_BODY8 \
    "s_waitcnt lgkmcnt(0)\n" \
    "v_mfma_f32_32x32x16_bf16 v[32:47], v[72:75], v[96:99], v[100:115]\n" \
    "ds_read_b128 v[72:75], v116 offset:4096\n" \
    "v_exp_f32 v120, v16\n" \
    "v_exp_f32 v121, v17\n" \
    "v_cvt_pk_bf16_f32 v71, v134, v135\n" \
    "v_exp_f32 v122, v18\n" \
    "v_exp_f32 v123, v19\n" \
    "v_cvt_pk_bf16_f32 v64, v120, v121\n" \
    "v_mfma_f32_32x32x16_bf16 v[48:63], v[92:95], v[68:71], v[48:63]\n" \
    "s_mov_b64 exec, s[42:43]\n" \
    "ds_read_b128 v[88:91], v116 offset:3072\n" \
    "ds_read_b128 v[92:95], v116 offset:3328\n" \
    "s_mov_b64 exec, -1\n" \
    "v_exp_f32 v124, v20\n" \
    "v_exp_f32 v125, v21\n" \
    "v_cvt_pk_bf16_f32 v65, v122, v123\n" \
    "v_exp_f32 v126, v22\n" \
    "v_exp_f32 v127, v23\n" \
    "v_cvt_pk_bf16_f32 v66, v124, v125\n" \
    "v_exp_f32 v128, v24\n" \
    "v_exp_f32 v129, v25\n" \
    "v_cvt_pk_bf16_f32 v67, v126, v127\n" \
    "v_exp_f32 v130, v26\n" \
    "v_exp_f32 v131, v27\n" \
    "v_mfma_f32_32x32x16_bf16 v[48:63], v[80:83], v[64:67], v[48:63]\n" \
    "v_exp_f32 v132, v28\n" \
    "v_exp_f32 v133, v29\n" \
    "v_cvt_pk_bf16_f32 v68, v128, v129\n" \
    "v_exp_f32 v134, v30\n" \
    "v_cvt_pk_bf16_f32 v69, v130, v131\n" \
    "v_exp_f32 v135, v31\n" \
    "v_cvt_pk_bf16_f32 v70, v132, v133\n" \
    "s_waitcnt lgkmcnt(0)\n" \
    "v_mfma_f32_32x32x16_bf16 v[16:31], v[72:75], v[96:99], v[100:115]\n" \
    "ds_read_b128 v[72:75], v116 offset:6144\n" \
    "v_exp_f32 v120, v32\n" \
    "v_exp_f32 v121, v33\n" \
    "v_cvt_pk_bf16_f32 v71, v134, v135\n" \
    "v_exp_f32 v122, v34\n" \
    "v_exp_f32 v123, v35\n" \
    "v_cvt_pk_bf16_f32 v64, v120, v121\n" \
    "v_mfma_f32_32x32x16_bf16 v[48:63], v[84:87], v[68:71], v[48:63]\n" \
    "s_mov_b64 exec, s[42:43]\n" \
    "ds_read_b128 v[80:83], v116 offset:5120\n" \
    "ds_read_b128 v[84:87], v116 offset:5376\n" \
    "s_mov_b64 exec, -1\n" \
    "v_exp_f32 v124, v36\n" \
    "v_exp_f32 v125, v37\n" \
    "v_cvt_pk_bf16_f32 v65, v122, v123\n" \
    "v_exp_f32 v126, v38\n" \
    "v_exp_f32 v127, v39\n" \
    "v_cvt_pk_bf16_f32 v66, v124, v125\n" \
    "v_exp_f32 v128, v40\n" \
    "v_exp_f32 v129, v41\n" \
    "v_cvt_pk_bf16_f32 v67, v126, v127\n" \
    "v_exp_f32 v130, v42\n" \
    "v_exp_f32 v131, v43\n" \
    "v_mfma_f32_32x32x16_bf16 v[48:63], v[88:91], v[64:67], v[48:63]\n" \
    "v_exp_f32 v132, v44\n" \
    "v_exp_f32 v133, v45\n" \
    "v_cvt_pk_bf16_f32 v68, v128, v129\n" \
    "v_exp_f32 v134, v46\n" \
    "v_cvt_pk_bf16_f32 v69, v130, v131\n" \
    "v_exp_f32 v135, v47\n" \
    "v_cvt_pk_bf16_f32 v70, v132, v133\n" \
    "s_waitcnt lgkmcnt(0)\n" \
    "v_mfma_f32_32x32x16_bf16 v[32:47], v[72:75], v[96:99], v[100:115]\n" \
    "ds_read_b128 v[72:75], v116 offset:8192\n" \
    "v_exp_f32 v120, v16\n" \
    "v_exp_f32 v121, v17\n" \
    "v_cvt_pk_bf16_f32 v71, v134, v135\n" \
    "v_exp_f32 v122, v18\n" \
    "v_exp_f32 v123, v19\n" \
    "v_cvt_pk_bf16_f32 v64, v120, v121\n" \
    "v_mfma_f32_32x32x16_bf16 v[48:63], v[92:95], v[68:71], v[48:63]\n" \
    "s_mov_b64 exec, s[42:43]\n" \
    "ds_read_b128 v[88:91], v116 offset:7168\n" \
    "ds_read_b128 v[92:95], v116 offset:7424\n" \
    "s_mov_b64 exec, -1\n" \
    "v_exp_f32 v124, v20\n" \
    "v_exp_f32 v125, v21\n" \
    "v_cvt_pk_bf16_f32 v65, v122, v123\n" \
    "v_exp_f32 v126, v22\n" \
    "v_exp_f32 v127, v23\n" \
    "v_cvt_pk_bf16_f32 v66, v124, v125\n" \
    "v_exp_f32 v128, v24\n" \
    "v_exp_f32 v129, v25\n" \
    "v_cvt_pk_bf16_f32 v67, v126, v127\n" \
    "v_exp_f32 v130, v26\n" \
    "v_exp_f32 v131, v27\n" \
    "v_mfma_f32_32x32x16_bf16 v[48:63], v[80:83], v[64:67], v[48:63]\n" \
    "v_exp_f32 v132, v28\n" \
    "v_exp_f32 v133, v29\n" \
    "v_cvt_pk_bf16_f32 v68, v128, v129\n" \
    "v_exp_f32 v134, v30\n" \
    "v_cvt_pk_bf16_f32 v69, v130, v131\n" \
    "v_exp_f32 v135, v31\n" \
    "v_cvt_pk_bf16_f32 v70, v132, v133\n" \
    "s_waitcnt lgkmcnt(0)\n" \
    "v_mfma_f32_32x32x16_bf16 v[16:31], v[72:75], v[96:99], v[100:115]\n" \
    "ds_read_b128 v[72:75], v116 offset:10240\n" \
    "v_exp_f32 v120, v32\n" \
    "v_exp_f32 v121, v33\n" \
    "v_cvt_pk_bf16_f32 v71, v134, v135\n" \
    "v_exp_f32 v122, v34\n" \
    "v_exp_f32 v123, v35\n" \
    "v_cvt_pk_bf16_f32 v64, v120, v121\n" \
    "v_mfma_f32_32x32x16_bf16 v[48:63], v[84:87], v[68:71], v[48:63]\n" \
    "s_mov_b64 exec, s[42:43]\n" \
    "ds_read_b128 v[80:83], v116 offset:9216\n" \
    "ds_read_b128 v[84:87], v116 offset:9472\n" \
    "s_mov_b64 exec, -1\n" \
    "v_exp_f32 v124, v36\n" \
    "v_exp_f32 v125, v37\n" \
    "v_cvt_pk_bf16_f32 v65, v122, v123\n" \
    "v_exp_f32 v126, v38\n" \
    "v_exp_f32 v127, v39\n" \
    "v_cvt_pk_bf16_f32 v66, v124, v125\n" \
    "v_exp_f32 v128, v40\n" \
    "v_exp_f32 v129, v41\n" \
    "v_cvt_pk_bf16_f32 v67, v126, v127\n" \
    "v_exp_f32 v130, v42\n" \
    "v_exp_f32 v131, v43\n" \
    "v_mfma_f32_32x32x16_bf16 v[48:63], v[88:91], v[64:67], v[48:63]\n" \
    "v_exp_f32 v132, v44\n" \
    "v_exp_f32 v133, v45\n" \
    "v_cvt_pk_bf16_f32 v68, v128, v129\n" \
    "v_exp_f32 v134, v46\n" \
    "v_cvt_pk_bf16_f32 v69, v130, v131\n" \
    "v_exp_f32 v135, v47\n" \
    "v_cvt_pk_bf16_f32 v70, v132, v133\n" \
    "s_waitcnt lgkmcnt(0)\n" \
    "v_mfma_f32_32x32x16_bf16 v[32:47], v[72:75], v[96:99], v[100:115]\n" \
    "ds_read_b128 v[72:75], v116 offset:12288\n" \
    "v_exp_f32 v120, v16\n" \
    "v_exp_f32 v121, v17\n" \
    "v_cvt_pk_bf16_f32 v71, v134, v135\n" \
    "v_exp_f32 v122, v18\n" \
    "v_exp_f32 v123, v19\n" \
    "v_cvt_pk_bf16_f32 v64, v120, v121\n" \
    "v_mfma_f32_32x32x16_bf16 v[48:63], v[92:95], v[68:71], v[48:63]\n" \
    "s_mov_b64 exec, s[42:43]\n" \
    "ds_read_b128 v[88:91], v116 offset:11264\n" \
    "ds_read_b128 v[92:95], v116 offset:11520\n" \
    "s_mov_b64 exec, -1\n" \
    "v_exp_f32 v124, v20\n" \
    "v_exp_f32 v125, v21\n" \
    "v_cvt_pk_bf16_f32 v65, v122, v123\n" \
    "v_exp_f32 v126, v22\n" \
    "v_exp_f32 v127, v23\n" \
    "v_cvt_pk_bf16_f32 v66, v124, v125\n" \
    "v_exp_f32 v128, v24\n" \
    "v_exp_f32 v129, v25\n" \
    "v_cvt_pk_bf16_f32 v67, v126, v127\n" \
    "v_exp_f32 v130, v26\n" \
    "v_exp_f32 v131, v27\n" \
    "v_mfma_f32_32x32x16_bf16 v[48:63], v[80:83], v[64:67], v[48:63]\n" \
    "v_exp_f32 v132, v28\n" \
    "v_exp_f32 v133, v29\n" \
    "v_cvt_pk_bf16_f32 v68, v128, v129\n" \
    "v_exp_f32 v134, v30\n" \
    "v_cvt_pk_bf16_f32 v69, v130, v131\n" \
    "v_exp_f32 v135, v31\n" \
    "v_cvt_pk_bf16_f32 v70, v132, v133\n" \
    "s_waitcnt lgkmcnt(0)\n" \
    "v_mfma_f32_32x32x16_bf16 v[16:31], v[72:75], v[96:99], v[100:115]\n" \
    "ds_read_b128 v[72:75], v116 offset:14336\n" \
    "v_exp_f32 v120, v32\n" \
    "v_exp_f32 v121, v33\n" \
    "v_cvt_pk_bf16_f32 v71, v134, v135\n" \
    "v_exp_f32 v122, v34\n" \
    "v_exp_f32 v123, v35\n" \
    "v_cvt_pk_bf16_f32 v64, v120, v121\n" \
    "v_mfma_f32_32x32x16_bf16 v[48:63], v[84:87], v[68:71], v[48:63]\n" \
    "s_mov_b64 exec, s[42:43]\n" \
    "ds_read_b128 v[80:83], v116 offset:13312\n" \
    "ds_read_b128 v[84:87], v116 offset:13568\n" \
    "s_mov_b64 exec, -1\n" \
    "v_exp_f32 v124, v36\n" \
    "v_exp_f32 v125, v37\n" \
    "v_cvt_pk_bf16_f32 v65, v122, v123\n" \
    "v_exp_f32 v126, v38\n" \
    "v_exp_f32 v127, v39\n" \
    "v_cvt_pk_bf16_f32 v66, v124, v125\n" \
    "v_exp_f32 v128, v40\n" \
    "v_exp_f32 v129, v41\n" \
    "v_cvt_pk_bf16_f32 v67, v126, v127\n" \
    "v_exp_f32 v130, v42\n" \
    "v_exp_f32 v131, v43\n" \
    "v_mfma_f32_32x32x16_bf16 v[48:63], v[88:91], v[64:67], v[48:63]\n" \
    "v_exp_f32 v132, v44\n" \
    "v_exp_f32 v133, v45\n" \
    "v_cvt_pk_bf16_f32 v68, v128, v129\n" \
    "v_exp_f32 v134, v46\n" \
    "v_cvt_pk_bf16_f32 v69, v130, v131\n" \
    "v_exp_f32 v135, v47\n" \
    "v_cvt_pk_bf16_f32 v70, v132, v133\n" \
    "s_waitcnt lgkmcnt(0)\n" \
    "v_mfma_f32_32x32x16_bf16 v[32:47], v[72:75], v[96:99], v[100:115]\n" \
    "ds_read_b128 v[72:75], v116 offset:16384\n" \
    "v_exp_f32 v120, v16\n" \
    "v_exp_f32 v121, v17\n" \
    "v_cvt_pk_bf16_f32 v71, v134, v135\n" \
    "v_exp_f32 v122, v18\n" \
    "v_exp_f32 v123, v19\n" \
    "v_cvt_pk_bf16_f32 v64, v120, v121\n" \
    "v_mfma_f32_32x32x16_bf16 v[48:63], v[92:95], v[68:71], v[48:63]\n" \
    "s_mov_b64 exec, s[42:43]\n" \
    "ds_read_b128 v[88:91], v116 offset:15360\n" \
    "ds_read_b128 v[92:95], v116 offset:15616\n" \
    "s_mov_b64 exec, -1\n" \
    "v_exp_f32 v124, v20\n" \
    "v_exp_f32 v125, v21\n" \
    "v_cvt_pk_bf16_f32 v65, v122, v123\n" \
    "v_exp_f32 v126, v22\n" \
    "v_exp_f32 v127, v23\n" \
    "v_cvt_pk_bf16_f32 v66, v124, v125\n" \
    "v_exp_f32 v128, v24\n" \
    "v_exp_f32 v129, v25\n" \
    "v_cvt_pk_bf16_f32 v67, v126, v127\n" \
    "v_exp_f32 v130, v26\n" \
    "v_exp_f32 v131, v27\n" \
    "v_mfma_f32_32x32x16_bf16 v[48:63], v[80:83], v[64:67], v[48:63]\n" \
    "v_exp_f32 v132, v28\n" \
    "v_exp_f32 v133, v29\n" \
    "v_cvt_pk_bf16_f32 v68, v128, v129\n" \
    "v_exp_f32 v134, v30\n" \
    "v_cvt_pk_bf16_f32 v69, v130, v131\n" \
    "v_exp_f32 v135, v31\n" \
    "v_cvt_pk_bf16_f32 v70, v132, v133\n" \
    "s_waitcnt lgkmcnt(0)\n" \
    "v_mfma_f32_32x32x16_bf16 v[16:31], v[72:75], v[96:99], v[100:115]\n" \
    "ds_read_b128 v[72:75], v116 offset:18432\n" \
    "v_exp_f32 v120, v32\n" \
    "v_exp_f32 v121, v33\n" \
    "v_cvt_pk_bf16_f32 v71, v134, v135\n" \
    "v_exp_f32 v122, v34\n" \
    "v_exp_f32 v123, v35\n" \
    "v_cvt_pk_bf16_f32 v64, v120, v121\n" \
    "v_mfma_f32_32x32x16_bf16 v[48:63], v[84:87], v[68:71], v[48:63]\n" \
    "s_mov_b64 exec, s[42:43]\n" \
    "ds_read_b128 v[80:83], v116 offset:17408\n" \
    "ds_read_b128 v[84:87], v116 offset:17664\n" \
    "s_mov_b64 exec, -1\n" \
    "v_exp_f32 v124, v36\n" \
    "v_exp_f32 v125, v37\n" \
    "v_cvt_pk_bf16_f32 v65, v122, v123\n" \
    "v_exp_f32 v126, v38\n" \
    "v_exp_f32 v127, v39\n" \
    "v_cvt_pk_bf16_f32 v66, v124, v125\n" \
    "v_exp_f32 v128, v40\n" \
    "v_exp_f32 v129, v41\n" \
    "v_cvt_pk_bf16_f32 v67, v126, v127\n" \
    "v_exp_f32 v130, v42\n" \
    "v_exp_f32 v131, v43\n" \
    "v_mfma_f32_32x32x16_bf16 v[48:63], v[88:91], v[64:67], v[48:63]\n" \
    "v_exp_f32 v132, v44\n" \
    "v_exp_f32 v133, v45\n" \
    "v_cvt_pk_bf16_f32 v68, v128, v129\n" \
    "v_exp_f32 v134, v46\n" \
    "v_cvt_pk_bf16_f32 v69, v130, v131\n" \
    "v_exp_f32 v135, v47\n" \
    "v_cvt_pk_bf16_f32 v70, v132, v133\n"

#define ATT_CLOBBERS "v16", "v17", "v18", "v19", "v20", "v21", "v22", "v23", "v24", "v25", "v26", "v27", "v28", "v29", "v30", "v31", "v32", "v33", "v34", "v35", "v36", "v37", "v38", "v39", "v40", "v41", "v42", "v43", "v44", "v45", "v46", "v47", "v64", "v65", "v66", "v67", "v68", "v69", "v70", "v71", "v72", "v73", "v74", "v75", "v76", "v77", "v78", "v79", "v80", "v81", "v82", "v83", "v84", "v85", "v86", "v87", "v88", "v89", "v90", "v91", "v92", "v93", "v94", "v95", "v100", "v101", "v102", "v103", "v104", "v105", "v106", "v107", "v108", "v109", "v110", "v111", "v112", "v113", "v114", "v115", "v116", "v120", "v121", "v122", "v123", "v124", "v125", "v126", "v127", "v128", "v129", "v130", "v131", "v132", "v133", "v134", "v135"

#define CONV_R0 \
    "ds_read_b64 v[52:53], v48 offset:736\n" \
    "ds_read_b64 v[54:55], v49 offset:736\n" \
    "ds_read_b128 v[68:71], v50 offset:3472\n" \
    "ds_read_b128 v[60:63], v50 offset:912\n" \
    "ds_read_b64 v[56:57], v48 offset:704\n" \
    "ds_read_b64 v[58:59], v49 offset:704\n" \
    "ds_read_b128 v[72:75], v50 offset:3440\n" \
    "ds_read_b128 v[64:67], v50 offset:880\n" \
    "s_waitcnt lgkmcnt(0)\n" \
    "v_mfma_f32_32x32x16_bf16 v[32:47], v[52:55], v[68:71], v[32:47]\n" \
    "ds_read_b64 v[76:77], v48 offset:672\n" \
    "ds_read_b64 v[78:79], v49 offset:672\n" \
    "v_mfma_f32_32x32x16_bf16 v[16:31], v[52:55], v[60:63], v[16:31]\n" \
    "ds_read_b128 v[92:95], v50 offset:3392\n" \
    "ds_read_b128 v[84:87], v50 offset:832\n" \
    "v_mfma_f32_32x32x16_bf16 v[32:47], v[56:59], v[72:75], v[32:47]\n" \
    "ds_read_b64 v[80:81], v48 offset:640\n" \
    "ds_read_b64 v[82:83], v49 offset:640\n" \
    "v_mfma_f32_32x32x16_bf16 v[16:31], v[56:59], v[64:67], v[16:31]\n" \
    "ds_read_b128 v[96:99], v50 offset:3360\n" \
    "ds_read_b128 v[88:91], v50 offset:800\n" \
    "s_waitcnt lgkmcnt(0)\n" \
    "v_mfma_f32_32x32x16_bf16 v[32:47], v[76:79], v[92:95], v[32:47]\n" \
    "ds_read_b64 v[52:53], v48 offset:608\n" \
    "ds_read_b64 v[54:55], v49 offset:608\n" \
    "v_mfma_f32_32x32x16_bf16 v[16:31], v[76:79], v[84:87], v[16:31]\n" \
    "ds_read_b128 v[68:71], v50 offset:3312\n" \
    "ds_read_b128 v[60:63], v50 offset:752\n" \
    "v_mfma_f32_32x32x16_bf16 v[32:47], v[80:83], v[96:99], v[32:47]\n" \
    "ds_read_b64 v[56:57], v48 offset:576\n" \
    "ds_read_b64 v[58:59], v49 offset:576\n" \
    "v_mfma_f32_32x32x16_bf16 v[16:31], v[80:83], v[88:91], v[16:31]\n" \
    "ds_read_b128 v[72:75], v50 offset:3280\n" \
    "ds_read_b128 v[64:67], v50 offset:720\n" \
    "s_waitcnt lgkmcnt(0)\n" \
    "v_mfma_f32_32x32x16_bf16 v[32:47], v[52:55], v[68:71], v[32:47]\n" \
    "ds_read_b64 v[76:77], v48 offset:544\n" \
    "ds_read_b64 v[78:79], v49 offset:544\n" \
    "v_mfma_f32_32x32x16_bf16 v[16:31], v[52:55], v[60:63], v[16:31]\n" \
    "ds_read_b128 v[92:95], v50 offset:3232\n" \
    "ds_read_b128 v[84:87], v50 offset:672\n" \
    "v_mfma_f32_32x32x16_bf16 v[32:47], v[56:59], v[72:75], v[32:47]\n" \
    "ds_read_b64 v[80:81], v48 offset:512\n" \
    "ds_read_b64 v[82:83], v49 offset:512\n" \
    "v_mfma_f32_32x32x16_bf16 v[16:31], v[56:59], v[64:67], v[16:31]\n" \
    "ds_read_b128 v[96:99], v50 offset:3200\n" \
    "ds_read_b128 v[88:91], v50 offset:640\n" \
    "s_waitcnt lgkmcnt(0)\n" \
    "v_mfma_f32_32x32x16_bf16 v[32:47], v[76:79], v[92:95], v[32:47]\n" \
    "ds_read_b64 v[52:53], v48 offset:480\n" \
    "ds_read_b64 v[54:55], v49 offset:480\n" \
    "v_mfma_f32_32x32x16_bf16 v[16:31], v[76:79], v[84:87], v[16:31]\n" \
    "ds_read_b128 v[68:71], v50 offset:3152\n" \
    "ds_read_b128 v[60:63], v50 offset:592\n" \
    "v_mfma_f32_32x32x16_bf16 v[32:47], v[80:83], v[96:99], v[32:47]\n" \
    "ds_read_b64 v[56:57], v48 offset:448\n" \
    "ds_read_b64 v[58:59], v49 offset:448\n" \
    "v_mfma_f32_32x32x16_bf16 v[16:31], v[80:83], v[88:91], v[16:31]\n" \
    "ds_read_b128 v[72:75], v50 offset:3120\n" \
    "ds_read_b128 v[64:67], v50 offset:560\n" \
    "s_waitcnt lgkmcnt(0)\n" \
    "v_mfma_f32_32x32x16_bf16 v[32:47], v[52:55], v[68:71], v[32:47]\n" \
    "ds_read_b64 v[76:77], v48 offset:416\n" \
    "ds_read_b64 v[78:79], v49 offset:416\n" \
    "v_mfma_f32_32x32x16_bf16 v[16:31], v[52:55], v[60:63], v[16:31]\n" \
    "ds_read_b128 v[92:95], v50 offset:3072\n" \
    "ds_read_b128 v[84:87], v50 offset:512\n" \
    "v_mfma_f32_32x32x16_bf16 v[32:47], v[56:59], v[72:75], v[32:47]\n" \
    "ds_read_b64 v[80:81], v48 offset:384\n" \
    "ds_read_b64 v[82:83], v49 offset:384\n" \
    "v_mfma_f32_32x32x16_bf16 v[16:31], v[56:59], v[64:67], v[16:31]\n" \
    "ds_read_b128 v[96:99], v50 offset:3040\n" \
    "ds_read_b128 v[88:91], v50 offset:480\n" \
    "s_waitcnt lgkmcnt(0)\n" \
    "v_mfma_f32_32x32x16_bf16 v[32:47], v[76:79], v[92:95], v[32:47]\n" \
    "ds_read_b64 v[52:53], v48 offset:352\n" \
    "ds_read_b64 v[54:55], v49 offset:352\n" \
    "v_mfma_f32_32x32x16_bf16 v[16:31], v[76:79], v[84:87], v[16:31]\n" \
    "ds_read_b128 v[68:71], v50 offset:2992\n" \
    "ds_read_b128 v[60:63], v50 offset:432\n" \
    "v_mfma_f32_32x32x16_bf16 v[32:47], v[80:83], v[96:99], v[32:47]\n" \
    "ds_read_b64 v[56:57], v48 offset:320\n" \
    "ds_read_b64 v[58:59], v49 offset:320\n" \
    "v_mfma_f32_32x32x16_bf16 v[16:31], v[80:83], v[88:91], v[16:31]\n" \
    "ds_read_b128 v[72:75], v50 offset:2960\n" \
    "ds_read_b128 v[64:67], v50 offset:400\n" \
    "s_waitcnt lgkmcnt(0)\n" \
    "v_mfma_f32_32x32x16_bf16 v[32:47], v[52:55], v[68:71], v[32:47]\n" \
    "ds_read_b64 v[76:77], v48 offset:288\n" \
    "ds_read_b64 v[78:79], v49 offset:288\n" \
    "v_mfma_f32_32x32x16_bf16 v[16:31], v[52:55], v[60:63], v[16:31]\n" \
    "ds_read_b128 v[92:95], v50 offset:2912\n" \
    "ds_read_b128 v[84:87], v50 offset:352\n" \
    "v_mfma_f32_32x32x16_bf16 v[32:47], v[56:59], v[72:75], v[32:47]\n" \
    "ds_read_b64 v[80:81], v48 offset:256\n" \
    "ds_read_b64 v[82:83], v49 offset:256\n" \
    "v_mfma_f32_32x32x16_bf16 v[16:31], v[56:59], v[64:67], v[16:31]\n" \
    "ds_read_b128 v[96:99], v50 offset:2880\n" \
    "ds_read_b128 v[88:91], v50 offset:320\n" \
    "s_waitcnt lgkmcnt(0)\n" \
    "v_mfma_f32_32x32x16_bf16 v[32:47], v[76:79], v[92:95], v[32:47]\n" \
    "ds_read_b64 v[52:53], v48 offset:224\n" \
    "ds_read_b64 v[54:55], v49 offset:224\n" \
    "v_mfma_f32_32x32x16_bf16 v[16:31], v[76:79], v[84:87], v[16:31]\n" \
    "ds_read_b128 v[68:71], v50 offset:2832\n" \
    "ds_read_b128 v[60:63], v50 offset:272\n" \
    "v_mfma_f32_32x32x16_bf16 v[32:47], v[80:83], v[96:99], v[32:47]\n" \
    "ds_read_b64 v[56:57], v48 offset:192\n" \
    "ds_read_b64 v[58:59], v49 offset:192\n" \
    "v_mfma_f32_32x32x16_bf16 v[16:31], v[80:83], v[88:91], v[16:31]\n" \
    "ds_read_b128 v[72:75], v50 offset:2800\n" \
    "ds_read_b128 v[64:67], v50 offset:240\n" \
    "s_waitcnt lgkmcnt(0)\n" \
    "v_mfma_f32_32x32x16_bf16 v[32:47], v[52:55], v[68:71], v[32:47]\n" \
    "ds_read_b64 v[76:77], v48 offset:160\n" \
    "ds_read_b64 v[78:79], v49 offset:160\n" \
    "v_mfma_f32_32x32x16_bf16 v[16:31], v[52:55], v[60:63], v[16:31]\n" \
    "ds_read_b128 v[92:95], v50 offset:2752\n" \
    "ds_read_b128 v[84:87], v50 offset:192\n" \
    "v_mfma_f32_32x32x16_bf16 v[32:47], v[56:59], v[72:75], v[32:47]\n" \
    "ds_read_b64 v[80:81], v48 offset:128\n" \
    "ds_read_b64 v[82:83], v49 offset:128\n" \
    "v_mfma_f32_32x32x16_bf16 v[16:31], v[56:59], v[64:67], v[16:31]\n" \
    "ds_read_b128 v[96:99], v50 offset:2720\n" \
    "ds_read_b128 v[88:91], v50 offset:160\n" \
    "s_waitcnt lgkmcnt(0)\n" \
    "v_mfma_f32_32x32x16_bf16 v[32:47], v[76:79], v[92:95], v[32:47]\n" \
    "ds_read_b64 v[52:53], v48 offset:96\n" \
    "ds_read_b64 v[54:55], v49 offset:96\n" \
    "v_mfma_f32_32x32x16_bf16 v[16:31], v[76:79], v[84:87], v[16:31]\n" \
    "ds_read_b128 v[68:71], v50 offset:2672\n" \
    "ds_read_b128 v[60:63], v50 offset:112\n" \
    "v_mfma_f32_32x32x16_bf16 v[32:47], v[80:83], v[96:99], v[32:47]\n" \
    "ds_read_b64 v[56:57], v48 offset:64\n" \
    "ds_read_b64 v[58:59], v49 offset:64\n" \
    "v_mfma_f32_32x32x16_bf16 v[16:31], v[80:83], v[88:91], v[16:31]\n" \
    "ds_read_b128 v[72:75], v50 offset:2640\n" \
    "ds_read_b128 v[64:67], v50 offset:80\n" \
    "s_waitcnt lgkmcnt(0)\n" \
    "v_mfma_f32_32x32x16_bf16 v[32:47], v[52:55], v[68:71], v[32:47]\n" \
    "ds_read_b64 v[76:77], v48 offset:32\n" \
    "ds_read_b64 v[78:79], v49 offset:32\n" \
    "v_mfma_f32_32x32x16_bf16 v[16:31], v[52:55], v[60:63], v[16:31]\n" \
    "ds_read_b128 v[92:95], v50 offset:2592\n" \
    "ds_read_b128 v[84:87], v50 offset:32\n" \
    "v_mfma_f32_32x32x16_bf16 v[32:47], v[56:59], v[72:75], v[32:47]\n" \
    "ds_read_b64 v[80:81], v48 offset:0\n" \
    "ds_read_b64 v[82:83], v49 offset:0\n" \
    "v_mfma_f32_32x32x16_bf16 v[16:31], v[56:59], v[64:67], v[16:31]\n" \
    "ds_read_b128 v[96:99], v50 offset:2560\n" \
    "ds_read_b128 v[88:91], v50 offset:0\n" \
    "s_waitcnt lgkmcnt(0)\n" \
    "v_mfma_f32_32x32x16_bf16 v[32:47], v[76:79], v[92:95], v[32:47]\n" \
    "v_mfma_f32_32x32x16_bf16 v[16:31], v[76:79], v[84:87], v[16:31]\n" \
    "v_mfma_f32_32x32x16_bf16 v[32:47], v[80:83], v[96:99], v[32:47]\n" \
    "v_mfma_f32_32x32x16_bf16 v[16:31], v[80:83], v[88:91], v[16:31]\n"

#define CONV_R2 \
    "ds_read_b64 v[52:53], v48 offset:992\n" \
    "ds_read_b64 v[54:55], v49 offset:992\n" \
    "ds_read_b128 v[68:71], v50 offset:3792\n" \
    "ds_read_b128 v[60:63], v50 offset:1232\n" \
    "ds_read_b64 v[56:57], v48 offset:960\n" \
    "ds_read_b64 v[58:59], v49 offset:960\n" \
    "ds_read_b128 v[72:75], v50 offset:3760\n" \
    "ds_read_b128 v[64:67], v50 offset:1200\n" \
    "s_waitcnt lgkmcnt(0)\n" \
    "v_mfma_f32_32x32x16_bf16 v[32:47], v[52:55], v[68:71], v[32:47]\n" \
    "ds_read_b64 v[76:77], v48 offset:928\n" \
    "ds_read_b64 v[78:79], v49 offset:928\n" \
    "v_mfma_f32_32x32x16_bf16 v[16:31], v[52:55], v[60:63], v[16:31]\n" \
    "ds_read_b128 v[92:95], v50 offset:3712\n" \
    "ds_read_b128 v[84:87], v50 offset:1152\n" \
    "v_mfma_f32_32x32x16_bf16 v[32:47], v[56:59], v[72:75], v[32:47]\n" \
    "ds_read_b64 v[80:81], v48 offset:896\n" \
    "ds_read_b64 v[82:83], v49 offset:896\n" \
    "v_mfma_f32_32x32x16_bf16 v[16:31], v[56:59], v[64:67], v[16:31]\n" \
    "ds_read_b128 v[96:99], v50 offset:3680\n" \
    "ds_read_b128 v[88:91], v50 offset:1120\n" \
    "s_waitcnt lgkmcnt(0)\n" \
    "v_mfma_f32_32x32x16_bf16 v[32:47], v[76:79], v[92:95], v[32:47]\n" \
    "ds_read_b64 v[52:53], v48 offset:864\n" \
    "ds_read_b64 v[54:55], v49 offset:864\n" \
    "v_mfma_f32_32x32x16_bf16 v[16:31], v[76:79], v[84:87], v[16:31]\n" \
    "ds_read_b128 v[68:71], v50 offset:3632\n" \
    "ds_read_b128 v[60:63], v50 offset:1072\n" \
    "v_mfma_f32_32x32x16_bf16 v[32:47], v[80:83], v[96:99], v[32:47]\n" \
    "ds_read_b64 v[56:57], v48 offset:832\n" \
    "ds_read_b64 v[58:59], v49 offset:832\n" \
    "v_mfma_f32_32x32x16_bf16 v[16:31], v[80:83], v[88:91], v[16:31]\n" \
    "ds_read_b128 v[72:75], v50 offset:3600\n" \
    "ds_read_b128 v[64:67], v50 offset:1040\n" \
    "s_waitcnt lgkmcnt(0)\n" \
    "v_mfma_f32_32x32x16_bf16 v[32:47], v[52:55], v[68:71], v[32:47]\n" \
    "ds_read_b64 v[76:77], v48 offset:800\n" \
    "ds_read_b64 v[78:79], v49 offset:800\n" \
    "v_mfma_f32_32x32x16_bf16 v[16:31], v[52:55], v[60:63], v[16:31]\n" \
    "ds_read_b128 v[92:95], v50 offset:3552\n" \
    "ds_read_b128 v[84:87], v50 offset:992\n" \
    "v_mfma_f32_32x32x16_bf16 v[32:47], v[56:59], v[72:75], v[32:47]\n" \
    "ds_read_b64 v[80:81], v48 offset:768\n" \
    "ds_read_b64 v[82:83], v49 offset:768\n" \
    "v_mfma_f32_32x32x16_bf16 v[16:31], v[56:59], v[64:67], v[16:31]\n" \
    "ds_read_b128 v[96:99], v50 offset:3520\n" \
    "ds_read_b128 v[88:91], v50 offset:960\n" \
    "s_waitcnt lgkmcnt(0)\n" \
    "v_mfma_f32_32x32x16_bf16 v[32:47], v[76:79], v[92:95], v[32:47]\n" \
    "ds_read_b64 v[52:53], v48 offset:736\n" \
    "ds_read_b64 v[54:55], v49 offset:736\n" \
    "v_mfma_f32_32x32x16_bf16 v[16:31], v[76:79], v[84:87], v[16:31]\n" \
    "ds_read_b128 v[68:71], v50 offset:3472\n" \
    "ds_read_b128 v[60:63], v50 offset:912\n" \
    "v_mfma_f32_32x32x16_bf16 v[32:47], v[80:83], v[96:99], v[32:47]\n" \
    "ds_read_b64 v[56:57], v48 offset:704\n" \
    "ds_read_b64 v[58:59], v49 offset:704\n" \
    "v_mfma_f32_32x32x16_bf16 v[16:31], v[80:83], v[88:91], v[16:31]\n" \
    "ds_read_b128 v[72:75], v50 offset:3440\n" \
    "ds_read_b128 v[64:67], v50 offset:880\n" \
    "s_waitcnt lgkmcnt(0)\n" \
    "v_mfma_f32_32x32x16_bf16 v[32:47], v[52:55], v[68:71], v[32:47]\n" \
    "ds_read_b64 v[76:77], v48 offset:672\n" \
    "ds_read_b64 v[78:79], v49 offset:672\n" \
    "v_mfma_f32_32x32x16_bf16 v[16:31], v[52:55], v[60:63], v[16:31]\n" \
    "ds_read_b128 v[92:95], v50 offset:3392\n" \
    "ds_read_b128 v[84:87], v50 offset:832\n" \
    "v_mfma_f32_32x32x16_bf16 v[32:47], v[56:59], v[72:75], v[32:47]\n" \
    "ds_read_b64 v[80:81], v48 offset:640\n" \
    "ds_read_b64 v[82:83], v49 offset:640\n" \
    "v_mfma_f32_32x32x16_bf16 v[16:31], v[56:59], v[64:67], v[16:31]\n" \
    "ds_read_b128 v[96:99], v50 offset:3360\n" \
    "ds_read_b128 v[88:91], v50 offset:800\n" \
    "s_waitcnt lgkmcnt(0)\n" \
    "v_mfma_f32_32x32x16_bf16 v[32:47], v[76:79], v[92:95], v[32:47]\n" \
    "ds_read_b64 v[52:53], v48 offset:608\n" \
    "ds_read_b64 v[54:55], v49 offset:608\n" \
    "v_mfma_f32_32x32x16_bf16 v[16:31], v[76:79], v[84:87], v[16:31]\n" \
    "ds_read_b128 v[68:71], v50 offset:3312\n" \
    "ds_read_b128 v[60:63], v50 offset:752\n" \
    "v_mfma_f32_32x32x16_bf16 v[32:47], v[80:83], v[96:99], v[32:47]\n" \
    "ds_read_b64 v[56:57], v48 offset:576\n" \
    "ds_read_b64 v[58:59], v49 offset:576\n" \
    "v_mfma_f32_32x32x16_bf16 v[16:31], v[80:83], v[88:91], v[16:31]\n" \
    "ds_read_b128 v[72:75], v50 offset:3280\n" \
    "ds_read_b128 v[64:67], v50 offset:720\n" \
    "s_waitcnt lgkmcnt(0)\n" \
    "v_mfma_f32_32x32x16_bf16 v[32:47], v[52:55], v[68:71], v[32:47]\n" \
    "ds_read_b64 v[76:77], v48 offset:544\n" \
    "ds_read_b64 v[78:79], v49 offset:544\n" \
    "v_mfma_f32_32x32x16_bf16 v[16:31], v[52:55], v[60:63], v[16:31]\n" \
    "ds_read_b128 v[92:95], v50 offset:3232\n" \
    "ds_read_b128 v[84:87], v50 offset:672\n" \
    "v_mfma_f32_32x32x16_bf16 v[32:47], v[56:59], v[72:75], v[32:47]\n" \
    "ds_read_b64 v[80:81], v48 offset:512\n" \
    "ds_read_b64 v[82:83], v49 offset:512\n" \
    "v_mfma_f32_32x32x16_bf16 v[16:31], v[56:59], v[64:67], v[16:31]\n" \
    "ds_read_b128 v[96:99], v50 offset:3200\n" \
    "ds_read_b128 v[88:91], v50 offset:640\n" \
    "s_waitcnt lgkmcnt(0)\n" \
    "v_mfma_f32_32x32x16_bf16 v[32:47], v[76:79], v[92:95], v[32:47]\n" \
    "ds_read_b64 v[52:53], v48 offset:480\n" \
    "ds_read_b64 v[54:55], v49 offset:480\n" \
    "v_mfma_f32_32x32x16_bf16 v[16:31], v[76:79], v[84:87], v[16:31]\n" \
    "ds_read_b128 v[68:71], v50 offset:3152\n" \
    "ds_read_b64 v[56:57], v48 offset:448\n" \
    "v_mfma_f32_32x32x16_bf16 v[32:47], v[80:83], v[96:99], v[32:47]\n" \
    "ds_read_b64 v[58:59], v49 offset:448\n" \
    "ds_read_b128 v[72:75], v50 offset:3120\n" \
    "v_mfma_f32_32x32x16_bf16 v[16:31], v[80:83], v[88:91], v[16:31]\n" \
    "s_waitcnt lgkmcnt(0)\n" \
    "v_mfma_f32_32x32x16_bf16 v[32:47], v[52:55], v[68:71], v[32:47]\n" \
    "ds_read_b64 v[76:77], v48 offset:416\n" \
    "ds_read_b64 v[78:79], v49 offset:416\n" \
    "ds_read_b128 v[92:95], v50 offset:3072\n" \
    "v_mfma_f32_32x32x16_bf16 v[32:47], v[56:59], v[72:75], v[32:47]\n" \
    "ds_read_b64 v[80:81], v48 offset:384\n" \
    "ds_read_b64 v[82:83], v49 offset:384\n" \
    "ds_read_b128 v[96:99], v50 offset:3040\n" \
    "s_waitcnt lgkmcnt(0)\n" \
    "v_mfma_f32_32x32x16_bf16 v[32:47], v[76:79], v[92:95], v[32:47]\n" \
    "ds_read_b64 v[52:53], v48 offset:352\n" \
    "ds_read_b64 v[54:55], v49 offset:352\n" \
    "ds_read_b128 v[68:71], v50 offset:2992\n" \
    "v_mfma_f32_32x32x16_bf16 v[32:47], v[80:83], v[96:99], v[32:47]\n" \
    "ds_read_b64 v[56:57], v48 offset:320\n" \
    "ds_read_b64 v[58:59], v49 offset:320\n" \
    "ds_read_b128 v[72:75], v50 offset:2960\n" \
    "s_waitcnt lgkmcnt(0)\n" \
    "v_mfma_f32_32x32x16_bf16 v[32:47], v[52:55], v[68:71], v[32:47]\n" \
    "ds_read_b64 v[76:77], v48 offset:288\n" \
    "ds_read_b64 v[78:79], v49 offset:288\n" \
    "ds_read_b128 v[92:95], v50 offset:2912\n" \
    "v_mfma_f32_32x32x16_bf16 v[32:47], v[56:59], v[72:75], v[32:47]\n" \
    "ds_read_b64 v[80:81], v48 offset:256\n" \
    "ds_read_b64 v[82:83], v49 offset:256\n" \
    "ds_read_b128 v[96:99], v50 offset:2880\n" \
    "s_waitcnt lgkmcnt(0)\n" \
    "v_mfma_f32_32x32x16_bf16 v[32:47], v[76:79], v[92:95], v[32:47]\n" \
    "ds_read_b64 v[52:53], v48 offset:224\n" \
    "ds_read_b64 v[54:55], v49 offset:224\n" \
    "ds_read_b128 v[68:71], v50 offset:2832\n" \
    "v_mfma_f32_32x32x16_bf16 v[32:47], v[80:83], v[96:99], v[32:47]\n" \
    "ds_read_b64 v[56:57], v48 offset:192\n" \
    "ds_read_b64 v[58:59], v49 offset:192\n" \
    "ds_read_b128 v[72:75], v50 offset:2800\n" \
    "s_waitcnt lgkmcnt(0)\n" \
    "v_mfma_f32_32x32x16_bf16 v[32:47], v[52:55], v[68:71], v[32:47]\n" \
    "ds_read_b64 v[76:77], v48 offset:160\n" \
    "ds_read_b64 v[78:79], v49 offset:160\n" \
    "ds_read_b128 v[92:95], v50 offset:2752\n" \
    "v_mfma_f32_32x32x16_bf16 v[32:47], v[56:59], v[72:75], v[32:47]\n" \
    "ds_read_b64 v[80:81], v48 offset:128\n" \
    "ds_read_b64 v[82:83], v49 offset:128\n" \
    "ds_read_b128 v[96:99], v50 offset:2720\n" \
    "s_waitcnt lgkmcnt(0)\n" \
    "v_mfma_f32_32x32x16_bf16 v[32:47], v[76:79], v[92:95], v[32:47]\n" \
    "ds_read_b64 v[52:53], v48 offset:96\n" \
    "ds_read_b64 v[54:55], v49 offset:96\n" \
    "ds_read_b128 v[68:71], v50 offset:2672\n" \
    "v_mfma_f32_32x32x16_bf16 v[32:47], v[80:83], v[96:99], v[32:47]\n" \
    "ds_read_b64 v[56:57], v48 offset:64\n" \
    "ds_read_b64 v[58:59], v49 offset:64\n" \
    "ds_read_b128 v[72:75], v50 offset:2640\n" \
    "s_waitcnt lgkmcnt(0)\n" \
    "v_mfma_f32_32x32x16_bf16 v[32:47], v[52:55], v[68:71], v[32:47]\n" \
    "ds_read_b64 v[76:77], v48 offset:32\n" \
    "ds_read_b64 v[78:79], v49 offset:32\n" \
    "ds_read_b128 v[92:95], v50 offset:2592\n" \
    "v_mfma_f32_32x32x16_bf16 v[32:47], v[56:59], v[72:75], v[32:47]\n" \
    "ds_read_b64 v[80:81], v48 offset:0\n" \
    "ds_read_b64 v[82:83], v49 offset:0\n" \
    "ds_read_b128 v[96:99], v50 offset:2560\n" \
    "s_waitcnt lgkmcnt(0)\n" \
    "v_mfma_f32_32x32x16_bf16 v[32:47], v[76:79], v[92:95], v[32:47]\n" \
    "v_mfma_f32_32x32x16_bf16 v[32:47], v[80:83], v[96:99], v[32:47]\n"

#define CONV_R3 \
    "ds_read_b64 v[52:53], v48 offset:1504\n" \
    "ds_read_b64 v[54:55], v49 offset:1504\n" \
    "ds_read_b128 v[68:71], v50 offset:4432\n" \
    "ds_read_b64 v[56:57], v48 offset:1472\n" \
    "ds_read_b64 v[58:59], v49 offset:1472\n" \
    "ds_read_b128 v[72:75], v50 offset:4400\n" \
    "s_waitcnt lgkmcnt(0)\n" \
    "v_mfma_f32_32x32x16_bf16 v[32:47], v[52:55], v[68:71], v[32:47]\n" \
    "ds_read_b64 v[76:77], v48 offset:1440\n" \
    "ds_read_b64 v[78:79], v49 offset:1440\n" \
    "ds_read_b128 v[92:95], v50 offset:4352\n" \
    "v_mfma_f32_32x32x16_bf16 v[32:47], v[56:59], v[72:75], v[32:47]\n" \
    "ds_read_b64 v[80:81], v48 offset:1408\n" \
    "ds_read_b64 v[82:83], v49 offset:1408\n" \
    "ds_read_b128 v[96:99], v50 offset:4320\n" \
    "s_waitcnt lgkmcnt(0)\n" \
    "v_mfma_f32_32x32x16_bf16 v[32:47], v[76:79], v[92:95], v[32:47]\n" \
    "ds_read_b64 v[52:53], v48 offset:1376\n" \
    "ds_read_b64 v[54:55], v49 offset:1376\n" \
    "ds_read_b128 v[68:71], v50 offset:4272\n" \
    "v_mfma_f32_32x32x16_bf16 v[32:47], v[80:83], v[96:99], v[32:47]\n" \
    "ds_read_b64 v[56:57], v48 offset:1344\n" \
    "ds_read_b64 v[58:59], v49 offset:1344\n" \
    "ds_read_b128 v[72:75], v50 offset:4240\n" \
    "s_waitcnt lgkmcnt(0)\n" \
    "v_mfma_f32_32x32x16_bf16 v[32:47], v[52:55], v[68:71], v[32:47]\n" \
    "ds_read_b64 v[76:77], v48 offset:1312\n" \
    "ds_read_b64 v[78:79], v49 offset:1312\n" \
    "ds_read_b128 v[92:95], v50 offset:4192\n" \
    "v_mfma_f32_32x32x16_bf16 v[32:47], v[56:59], v[72:75], v[32:47]\n" \
    "ds_read_b64 v[80:81], v48 offset:1280\n" \
    "ds_read_b64 v[82:83], v49 offset:1280\n" \
    "ds_read_b128 v[96:99], v50 offset:4160\n" \
    "s_waitcnt lgkmcnt(0)\n" \
    "v_mfma_f32_32x32x16_bf16 v[32:47], v[76:79], v[92:95], v[32:47]\n" \
    "ds_read_b64 v[52:53], v48 offset:1248\n" \
    "ds_read_b64 v[54:55], v49 offset:1248\n" \
    "ds_read_b128 v[68:71], v50 offset:4112\n" \
    "v_mfma_f32_32x32x16_bf16 v[32:47], v[80:83], v[96:99], v[32:47]\n" \
    "ds_read_b64 v[56:57], v48 offset:1216\n" \
    "ds_read_b64 v[58:59], v49 offset:1216\n" \
    "ds_read_b128 v[72:75], v50 offset:4080\n" \
    "s_waitcnt lgkmcnt(0)\n" \
    "v_mfma_f32_32x32x16_bf16 v[32:47], v[52:55], v[68:71], v[32:47]\n" \
    "ds_read_b64 v[76:77], v48 offset:1184\n" \
    "ds_read_b64 v[78:79], v49 offset:1184\n" \
    "ds_read_b128 v[92:95], v50 offset:4032\n" \
    "v_mfma_f32_32x32x16_bf16 v[32:47], v[56:59], v[72:75], v[32:47]\n" \
    "ds_read_b64 v[80:81], v48 offset:1152\n" \
    "ds_read_b64 v[82:83], v49 offset:1152\n" \
    "ds_read_b128 v[96:99], v50 offset:4000\n" \
    "s_waitcnt lgkmcnt(0)\n" \
    "v_mfma_f32_32x32x16_bf16 v[32:47], v[76:79], v[92:95], v[32:47]\n" \
    "ds_read_b64 v[52:53], v48 offset:1120\n" \
    "ds_read_b64 v[54:55], v49 offset:1120\n" \
    "ds_read_b128 v[68:71], v50 offset:3952\n" \
    "v_mfma_f32_32x32x16_bf16 v[32:47], v[80:83], v[96:99], v[32:47]\n" \
    "ds_read_b64 v[56:57], v48 offset:1088\n" \
    "ds_read_b64 v[58:59], v49 offset:1088\n" \
    "ds_read_b128 v[72:75], v50 offset:3920\n" \
    "s_waitcnt lgkmcnt(0)\n" \
    "v_mfma_f32_32x32x16_bf16 v[32:47], v[52:55], v[68:71], v[32:47]\n" \
    "ds_read_b64 v[76:77], v48 offset:1056\n" \
    "ds_read_b64 v[78:79], v49 offset:1056\n" \
    "ds_read_b128 v[92:95], v50 offset:3872\n" \
    "v_mfma_f32_32x32x16_bf16 v[32:47], v[56:59], v[72:75], v[32:47]\n" \
    "ds_read_b64 v[80:81], v48 offset:1024\n" \
    "ds_read_b64 v[82:83], v49 offset:1024\n" \
    "ds_read_b128 v[96:99], v50 offset:3840\n" \
    "s_waitcnt lgkmcnt(0)\n" \
    "v_mfma_f32_32x32x16_bf16 v[32:47], v[76:79], v[92:95], v[32:47]\n" \
    "ds_read_b64 v[52:53], v48 offset:992\n" \
    "ds_read_b64 v[54:55], v49 offset:992\n" \
    "ds_read_b128 v[68:71], v50 offset:3792\n" \
    "v_mfma_f32_32x32x16_bf16 v[32:47], v[80:83], v[96:99], v[32:47]\n" \
    "ds_read_b64 v[56:57], v48 offset:960\n" \
    "ds_read_b64 v[58:59], v49 offset:960\n" \
    "ds_read_b128 v[72:75], v50 offset:3760\n" \
    "s_waitcnt lgkmcnt(0)\n" \
    "v_mfma_f32_32x32x16_bf16 v[32:47], v[52:55], v[68:71], v[32:47]\n" \
    "ds_read_b64 v[76:77], v48 offset:928\n" \
    "ds_read_b64 v[78:79], v49 offset:928\n" \
    "ds_read_b128 v[92:95], v50 offset:3712\n" \
    "v_mfma_f32_32x32x16_bf16 v[32:47], v[56:59], v[72:75], v[32:47]\n" \
    "ds_read_b64 v[80:81], v48 offset:896\n" \
    "ds_read_b64 v[82:83], v49 offset:896\n" \
    "ds_read_b128 v[96:99], v50 offset:3680\n" \
    "s_waitcnt lgkmcnt(0)\n" \
    "v_mfma_f32_32x32x16_bf16 v[32:47], v[76:79], v[92:95], v[32:47]\n" \
    "ds_read_b64 v[52:53], v48 offset:864\n" \
    "ds_read_b64 v[54:55], v49 offset:864\n" \
    "ds_read_b128 v[68:71], v50 offset:3632\n" \
    "v_mfma_f32_32x32x16_bf16 v[32:47], v[80:83], v[96:99], v[32:47]\n" \
    "ds_read_b64 v[56:57], v48 offset:832\n" \
    "ds_read_b64 v[58:59], v49 offset:832\n" \
    "ds_read_b128 v[72:75], v50 offset:3600\n" \
    "s_waitcnt lgkmcnt(0)\n" \
    "v_mfma_f32_32x32x16_bf16 v[32:47], v[52:55], v[68:71], v[32:47]\n" \
    "ds_read_b64 v[76:77], v48 offset:800\n" \
    "ds_read_b64 v[78:79], v49 offset:800\n" \
    "ds_read_b128 v[92:95], v50 offset:3552\n" \
    "v_mfma_f32_32x32x16_bf16 v[32:47], v[56:59], v[72:75], v[32:47]\n" \
    "ds_read_b64 v[80:81], v48 offset:768\n" \
    "ds_read_b64 v[82:83], v49 offset:768\n" \
    "ds_read_b128 v[96:99], v50 offset:3520\n" \
    "s_waitcnt lgkmcnt(0)\n" \
    "v_mfma_f32_32x32x16_bf16 v[32:47], v[76:79], v[92:95], v[32:47]\n" \
    "ds_read_b64 v[52:53], v48 offset:736\n" \
    "ds_read_b64 v[54:55], v49 offset:736\n" \
    "ds_read_b128 v[68:71], v50 offset:3472\n" \
    "v_mfma_f32_32x32x16_bf16 v[32:47], v[80:83], v[96:99], v[32:47]\n" \
    "ds_read_b64 v[56:57], v48 offset:704\n" \
    "ds_read_b64 v[58:59], v49 offset:704\n" \
    "ds_read_b128 v[72:75], v50 offset:3440\n" \
    "s_waitcnt lgkmcnt(0)\n" \
    "v_mfma_f32_32x32x16_bf16 v[32:47], v[52:55], v[68:71], v[32:47]\n" \
    "ds_read_b64 v[76:77], v48 offset:672\n" \
    "ds_read_b64 v[78:79], v49 offset:672\n" \
    "ds_read_b128 v[92:95], v50 offset:3392\n" \
    "v_mfma_f32_32x32x16_bf16 v[32:47], v[56:59], v[72:75], v[32:47]\n" \
    "ds_read_b64 v[80:81], v48 offset:640\n" \
    "ds_read_b64 v[82:83], v49 offset:640\n" \
    "ds_read_b128 v[96:99], v50 offset:3360\n" \
    "s_waitcnt lgkmcnt(0)\n" \
    "v_mfma_f32_32x32x16_bf16 v[32:47], v[76:79], v[92:95], v[32:47]\n" \
    "ds_read_b64 v[52:53], v48 offset:608\n" \
    "ds_read_b64 v[54:55], v49 offset:608\n" \
    "ds_read_b128 v[68:71], v50 offset:3312\n" \
    "v_mfma_f32_32x32x16_bf16 v[32:47], v[80:83], v[96:99], v[32:47]\n" \
    "ds_read_b64 v[56:57], v48 offset:576\n" \
    "ds_read_b64 v[58:59], v49 offset:576\n" \
    "ds_read_b128 v[72:75], v50 offset:3280\n" \
    "s_waitcnt lgkmcnt(0)\n" \
    "v_mfma_f32_32x32x16_bf16 v[32:47], v[52:55], v[68:71], v[32:47]\n" \
    "ds_read_b64 v[76:77], v48 offset:544\n" \
    "ds_read_b64 v[78:79], v49 offset:544\n" \
    "ds_read_b128 v[92:95], v50 offset:3232\n" \
    "v_mfma_f32_32x32x16_bf16 v[32:47], v[56:59], v[72:75], v[32:47]\n" \
    "ds_read_b64 v[80:81], v48 offset:512\n" \
    "ds_read_b64 v[82:83], v49 offset:512\n" \
    "ds_read_b128 v[96:99], v50 offset:3200\n" \
    "s_waitcnt lgkmcnt(0)\n" \
    "v_mfma_f32_32x32x16_bf16 v[32:47], v[76:79], v[92:95], v[32:47]\n" \
    "ds_read_b64 v[52:53], v48 offset:480\n" \
    "ds_read_b64 v[54:55], v49 offset:480\n" \
    "ds_read_b128 v[68:71], v50 offset:3152\n" \
    "v_mfma_f32_32x32x16_bf16 v[32:47], v[80:83], v[96:99], v[32:47]\n" \
    "ds_read_b64 v[56:57], v48 offset:448\n" \
    "ds_read_b64 v[58:59], v49 offset:448\n" \
    "ds_read_b128 v[72:75], v50 offset:3120\n" \
    "s_waitcnt lgkmcnt(0)\n" \
    "v_mfma_f32_32x32x16_bf16 v[32:47], v[52:55], v[68:71], v[32:47]\n" \
    "ds_read_b64 v[76:77], v48 offset:416\n" \
    "ds_read_b64 v[78:79], v49 offset:416\n" \
    "ds_read_b128 v[92:95], v50 offset:3072\n" \
    "v_mfma_f32_32x32x16_bf16 v[32:47], v[56:59], v[72:75], v[32:47]\n" \
    "ds_read_b64 v[80:81], v48 offset:384\n" \
    "ds_read_b64 v[82:83], v49 offset:384\n" \
    "ds_read_b128 v[96:99], v50 offset:3040\n" \
    "s_waitcnt lgkmcnt(0)\n" \
    "v_mfma_f32_32x32x16_bf16 v[32:47], v[76:79], v[92:95], v[32:47]\n" \
    "ds_read_b64 v[52:53], v48 offset:352\n" \
    "ds_read_b64 v[54:55], v49 offset:352\n" \
    "ds_read_b128 v[68:71], v50 offset:2992\n" \
    "v_mfma_f32_32x32x16_bf16 v[32:47], v[80:83], v[96:99], v[32:47]\n" \
    "ds_read_b64 v[56:57], v48 offset:320\n" \
    "ds_read_b64 v[58:59], v49 offset:320\n" \
    "ds_read_b128 v[72:75], v50 offset:2960\n" \
    "s_waitcnt lgkmcnt(0)\n" \
    "v_mfma_f32_32x32x16_bf16 v[32:47], v[52:55], v[68:71], v[32:47]\n" \
    "ds_read_b64 v[76:77], v48 offset:288\n" \
    "ds_read_b64 v[78:79], v49 offset:288\n" \
    "ds_read_b128 v[92:95], v50 offset:2912\n" \
    "v_mfma_f32_32x32x16_bf16 v[32:47], v[56:59], v[72:75], v[32:47]\n" \
    "ds_read_b64 v[80:81], v48 offset:256\n" \
    "ds_read_b64 v[82:83], v49 offset:256\n" \
    "ds_read_b128 v[96:99], v50 offset:2880\n" \
    "s_waitcnt lgkmcnt(0)\n" \
    "v_mfma_f32_32x32x16_bf16 v[32:47], v[76:79], v[92:95], v[32:47]\n" \
    "ds_read_b64 v[52:53], v48 offset:224\n" \
    "ds_read_b64 v[54:55], v49 offset:224\n" \
    "ds_read_b128 v[68:71], v50 offset:2832\n" \
    "v_mfma_f32_32x32x16_bf16 v[32:47], v[80:83], v[96:99], v[32:47]\n" \
    "ds_read_b64 v[56:57], v48 offset:192\n" \
    "ds_read_b64 v[58:59], v49 offset:192\n" \
    "ds_read_b128 v[72:75], v50 offset:2800\n" \
    "s_waitcnt lgkmcnt(0)\n" \
    "v_mfma_f32_32x32x16_bf16 v[32:47], v[52:55], v[68:71], v[32:47]\n" \
    "ds_read_b64 v[76:77], v48 offset:160\n" \
    "ds_read_b64 v[78:79], v49 offset:160\n" \
    "ds_read_b128 v[92:95], v50 offset:2752\n" \
    "v_mfma_f32_32x32x16_bf16 v[32:47], v[56:59], v[72:75], v[32:47]\n" \
    "ds_read_b64 v[80:81], v48 offset:128\n" \
    "ds_read_b64 v[82:83], v49 offset:128\n" \
    "ds_read_b128 v[96:99], v50 offset:2720\n" \
    "s_waitcnt lgkmcnt(0)\n" \
    "v_mfma_f32_32x32x16_bf16 v[32:47], v[76:79], v[92:95], v[32:47]\n" \
    "ds_read_b64 v[52:53], v48 offset:96\n" \
    "ds_read_b64 v[54:55], v49 offset:96\n" \
    "ds_read_b128 v[68:71], v50 offset:2672\n" \
    "v_mfma_f32_32x32x16_bf16 v[32:47], v[80:83], v[96:99], v[32:47]\n" \
    "ds_read_b64 v[56:57], v48 offset:64\n" \
    "ds_read_b64 v[58:59], v49 offset:64\n" \
    "ds_read_b128 v[72:75], v50 offset:2640\n" \
    "s_waitcnt lgkmcnt(0)\n" \
    "v_mfma_f32_32x32x16_bf16 v[32:47], v[52:55], v[68:71], v[32:47]\n" \
    "ds_read_b64 v[76:77], v48 offset:32\n" \
    "ds_read_b64 v[78:79], v49 offset:32\n" \
    "ds_read_b128 v[92:95], v50 offset:2592\n" \
    "v_mfma_f32_32x32x16_bf16 v[32:47], v[56:59], v[72:75], v[32:47]\n" \
    "ds_read_b64 v[80:81], v48 offset:0\n" \
    "ds_read_b64 v[82:83], v49 offset:0\n" \
    "ds_read_b128 v[96:99], v50 offset:2560\n" \
    "s_waitcnt lgkmcnt(0)\n" \
    "v_mfma_f32_32x32x16_bf16 v[32:47], v[76:79], v[92:95], v[32:47]\n" \
    "v_mfma_f32_32x32x16_bf16 v[32:47], v[80:83], v[96:99], v[32:47]\n"

#define CONV_CLOBBERS "v52", "v53", "v54", "v55", "v56", "v57", "v58", "v59", "v60", "v61", "v62", "v63", "v64", "v65", "v66", "v67", "v68", "v69", "v70", "v71", "v72", "v73", "v74", "v75", "v76", "v77", "v78", "v79", "v80", "v81", "v82", "v83", "v84", "v85", "v86", "v87", "v88", "v89", "v90", "v91", "v92", "v93", "v94", "v95", "v96", "v97", "v98", "v99"

#define QKV_STEP \
    "ds_read_b128 v[64:67], %[x0]\n" \
    "ds_read_b64_tr_b16 v[68:69], %[wa] offset:0\n" \
    "ds_read_b64_tr_b16 v[70:71], %[wa] offset:512\n" \
    "ds_read_b64_tr_b16 v[72:73], %[wa] offset:8192\n" \
    "ds_read_b64_tr_b16 v[74:75], %[wa] offset:8704\n" \
    "ds_read_b64_tr_b16 v[76:77], %[wa] offset:16384\n" \
    "ds_read_b64_tr_b16 v[78:79], %[wa] offset:16896\n" \
    "ds_read_b128 v[80:83], %[x1]\n" \
    "ds_read_b64_tr_b16 v[84:85], %[wa] offset:2048\n" \
    "ds_read_b64_tr_b16 v[86:87], %[wa] offset:2560\n" \
    "ds_read_b64_tr_b16 v[88:89], %[wa] offset:10240\n" \
    "ds_read_b64_tr_b16 v[90:91], %[wa] offset:10752\n" \
    "ds_read_b64_tr_b16 v[92:93], %[wa] offset:18432\n" \
    "ds_read_b64_tr_b16 v[94:95], %[wa] offset:18944\n" \
    "s_waitcnt lgkmcnt(7)\n" \
    "v_mfma_f32_32x32x16_bf16 v[16:31], v[68:71], v[64:67], v[16:31]\n" \
    "ds_read_b64_tr_b16 v[68:69], %[wa] offset:4096\n" \
    "ds_read_b64_tr_b16 v[70:71], %[wa] offset:4608\n" \
    "v_mfma_f32_32x32x16_bf16 v[32:47], v[72:75], v[64:67], v[32:47]\n" \
    "ds_read_b64_tr_b16 v[72:73], %[wa] offset:12288\n" \
    "ds_read_b64_tr_b16 v[74:75], %[wa] offset:12800\n" \
    "v_mfma_f32_32x32x16_bf16 v[48:63], v[64:67], v[76:79], v[48:63]\n" \
    "ds_read_b128 v[64:67], %[x2]\n" \
    "ds_read_b64_tr_b16 v[76:77], %[wa] offset:20480\n" \
    "ds_read_b64_tr_b16 v[78:79], %[wa] offset:20992\n" \
    "s_waitcnt lgkmcnt(7)\n" \
    "v_mfma_f32_32x32x16_bf16 v[16:31], v[84:87], v[80:83], v[16:31]\n" \
    "ds_read_b64_tr_b16 v[84:85], %[wa] offset:6144\n" \
    "ds_read_b64_tr_b16 v[86:87], %[wa] offset:6656\n" \
    "v_mfma_f32_32x32x16_bf16 v[32:47], v[88:91], v[80:83], v[32:47]\n" \
    "ds_read_b64_tr_b16 v[88:89], %[wa] offset:14336\n" \
    "ds_read_b64_tr_b16 v[90:91], %[wa] offset:14848\n" \
    "v_mfma_f32_32x32x16_bf16 v[48:63], v[80:83], v[92:95], v[48:63]\n" \
    "ds_read_b128 v[80:83], %[x3]\n" \
    "ds_read_b64_tr_b16 v[92:93], %[wa] offset:22528\n" \
    "ds_read_b64_tr_b16 v[94:95], %[wa] offset:23040\n" \
    "s_waitcnt lgkmcnt(7)\n" \
    "v_mfma_f32_32x32x16_bf16 v[16:31], v[68:71], v[64:67], v[16:31]\n" \
    "v_mfma_f32_32x32x16_bf16 v[32:47], v[72:75], v[64:67], v[32:47]\n" \
    "v_mfma_f32_32x32x16_bf16 v[48:63], v[64:67], v[76:79], v[48:63]\n" \
    "s_waitcnt lgkmcnt(0)\n" \
    "v_mfma_f32_32x32x16_bf16 v[16:31], v[84:87], v[80:83], v[16:31]\n" \
    "v_mfma_f32_32x32x16_bf16 v[32:47], v[88:91], v[80:83], v[32:47]\n" \
    "v_mfma_f32_32x32x16_bf16 v[48:63], v[80:83], v[92:95], v[48:63]\n"

#define QKV_CLOBBERS "v64", "v65", "v66", "v67", "v68", "v69", "v70", "v71", "v72", "v73", "v74", "v75", "v76", "v77", "v78", "v79", "v80", "v81", "v82", "v83", "v84", "v85", "v86", "v87", "v88", "v89", "v90", "v91", "v92", "v93", "v94", "v95"

#define GEN_PAIR \
    "v_cvt_pk_f16_f32 %0, %16, -%17\n" \
    "v_cvt_pk_f16_f32 %8, %18, -%19\n" \
    "v_mul_f32 %20, %17, %17\n" \
    "v_mul_f32 %22, %19, %19\n" \
    "v_add_f32 %21, %16, %16\n" \
    "v_add_f32 %23, %18, %18\n" \
    "v_fma_f32 %16, %16, %16, -%20\n" \
    "v_fma_f32 %18, %18, %18, -%22\n" \
    "v_mul_f32 %17, %21, %17\n" \
    "v_mul_f32 %19, %23, %19\n" \
    "v_cvt_pk_f16_f32 %1, %16, -%17\n" \
    "v_cvt_pk_f16_f32 %9, %18, -%19\n" \
    "v_mul_f32 %20, %17, %17\n" \
    "v_mul_f32 %22, %19, %19\n" \
    "v_add_f32 %21, %16, %16\n" \
    "v_add_f32 %23, %18, %18\n" \
    "v_fma_f32 %16, %16, %16, -%20\n" \
    "v_fma_f32 %18, %18, %18, -%22\n" \
    "v_mul_f32 %17, %21, %17\n" \
    "v_mul_f32 %19, %23, %19\n" \
    "v_cvt_pk_f16_f32 %2, %16, -%17\n" \
    "v_cvt_pk_f16_f32 %10, %18, -%19\n" \
    "v_mul_f32 %20, %17, %17\n" \
    "v_mul_f32 %22, %19, %19\n" \
    "v_add_f32 %21, %16, %16\n" \
    "v_add_f32 %23, %18, %18\n" \
    "v_fma_f32 %16, %16, %16, -%20\n" \
    "v_fma_f32 %18, %18, %18, -%22\n" \
    "v_mul_f32 %17, %21, %17\n" \
    "v_mul_f32 %19, %23, %19\n" \
    "v_cvt_pk_f16_f32 %3, %16, -%17\n" \
    "v_cvt_pk_f16_f32 %11, %18, -%19\n" \
    "v_mul_f32 %20, %17, %17\n" \
    "v_mul_f32 %22, %19, %19\n" \
    "v_add_f32 %21, %16, %16\n" \
    "v_add_f32 %23, %18, %18\n" \
    "v_fma_f32 %16, %16, %16, -%20\n" \
    "v_fma_f32 %18, %18, %18, -%22\n" \
    "v_mul_f32 %17, %21, %17\n" \
    "v_mul_f32 %19, %23, %19\n" \
    "v_mul_f32 %20, %24, %16\n" \
    "v_mul_f32 %22, %26, %18\n" \
    "v_mul_f32 %21, %24, %17\n" \
    "v_mul_f32 %23, %26, %19\n" \
    "v_fma_f32 %20, -%25, %17, %20\n" \
    "v_fma_f32 %22, -%27, %19, %22\n" \
    "v_fma_f32 %21, %25, %16, %21\n" \
    "v_fma_f32 %23, %27, %18, %23\n" \
    "v_cvt_pk_f16_f32 %4, %20, %21\n" \
    "v_cvt_pk_f16_f32 %12, %22, %23\n" \
    "v_mul_f32 %20, %17, %17\n" \
    "v_mul_f32 %22, %19, %19\n" \
    "v_add_f32 %21, %16, %16\n" \
    "v_add_f32 %23, %18, %18\n" \
    "v_fma_f32 %16, %16, %16, -%20\n" \
    "v_fma_f32 %18, %18, %18, -%22\n" \
    "v_mul_f32 %17, %21, %17\n" \
    "v_mul_f32 %19, %23, %19\n" \
    "v_mul_f32 %20, %24, %16\n" \
    "v_mul_f32 %22, %26, %18\n" \
    "v_mul_f32 %21, %24, %17\n" \
    "v_mul_f32 %23, %26, %19\n" \
    "v_fma_f32 %20, -%25, %17, %20\n" \
    "v_fma_f32 %22, -%27, %19, %22\n" \
    "v_fma_f32 %21, %25, %16, %21\n" \
    "v_fma_f32 %23, %27, %18, %23\n" \
    "v_cvt_pk_f16_f32 %5, %20, %21\n" \
    "v_cvt_pk_f16_f32 %13, %22, %23\n" \
    "v_mul_f32 %20, %17, %17\n" \
    "v_mul_f32 %22, %19, %19\n" \
    "v_add_f32 %21, %16, %16\n" \
    "v_add_f32 %23, %18, %18\n" \
    "v_fma_f32 %16, %16, %16, -%20\n" \
    "v_fma_f32 %18, %18, %18, -%22\n" \
    "v_mul_f32 %17, %21, %17\n" \
    "v_mul_f32 %19, %23, %19\n" \
    "v_mul_f32 %20, %24, %16\n" \
    "v_mul_f32 %22, %26, %18\n" \
    "v_mul_f32 %21, %24, %17\n" \
    "v_mul_f32 %23, %26, %19\n" \
    "v_fma_f32 %20, -%25, %17, %20\n" \
    "v_fma_f32 %22, -%27, %19, %22\n" \
    "v_fma_f32 %21, %25, %16, %21\n" \
    "v_fma_f32 %23, %27, %18, %23\n" \
    "v_cvt_pk_f16_f32 %6, %20, %21\n" \
    "v_cvt_pk_f16_f32 %14, %22, %23\n" \
    "v_mul_f32 %20, %17, %17\n" \
    "v_mul_f32 %22, %19, %19\n" \
    "v_add_f32 %21, %16, %16\n" \
    "v_add_f32 %23, %18, %18\n" \
    "v_fma_f32 %16, %16, %16, -%20\n" \
    "v_fma_f32 %18, %18, %18, -%22\n" \
    "v_mul_f32 %17, %21, %17\n" \
    "v_mul_f32 %19, %23, %19\n" \
    "v_mul_f32 %20, %24, %16\n" \
    "v_mul_f32 %22, %26, %18\n" \
    "v_mul_f32 %21, %24, %17\n" \
    "v_mul_f32 %23, %26, %19\n" \
    "v_fma_f32 %20, -%25, %17, %20\n" \
    "v_fma_f32 %22, -%27, %19, %22\n" \
    "v_fma_f32 %21, %25, %16, %21\n" \
    "v_fma_f32 %23, %27, %18, %23\n" \
    "v_cvt_pk_f16_f32 %7, %20, %21\n" \
    "v_cvt_pk_f16_f32 %15, %22, %23\n" \
    "s_nop 1\n"

#define OPROJ_MFMA \
    "ds_read_b64_tr_b16 v[32:33], %[wa] offset:0\n" \
    "ds_read_b64_tr_b16 v[34:35], %[wa] offset:1024\n" \
    "ds_read_b128 v[36:39], %[oa] offset:0\n" \
    "ds_read_b64_tr_b16 v[40:41], %[wa] offset:2048\n" \
    "ds_read_b64_tr_b16 v[42:43], %[wa] offset:3072\n" \
    "ds_read_b128 v[44:47], %[oa] offset:1024\n" \
    "ds_read_b64_tr_b16 v[48:49], %[wa] offset:4096\n" \
    "ds_read_b64_tr_b16 v[50:51], %[wa] offset:5120\n" \
    "ds_read_b128 v[52:55], %[oa] offset:2048\n" \
    "ds_read_b64_tr_b16 v[56:57], %[wa] offset:6144\n" \
    "ds_read_b64_tr_b16 v[58:59], %[wa] offset:7168\n" \
    "ds_read_b128 v[60:63], %[oa] offset:3072\n" \
    "s_waitcnt lgkmcnt(9)\n" \
    "v_mfma_f32_32x32x16_bf16 v[16:31], v[32:35], v[36:39], 0\n" \
    "ds_read_b64_tr_b16 v[32:33], %[wa] offset:8192\n" \
    "ds_read_b64_tr_b16 v[34:35], %[wa] offset:9216\n" \
    "ds_read_b128 v[36:39], %[oa] offset:4096\n" \
    "s_waitcnt lgkmcnt(9)\n" \
    "v_mfma_f32_32x32x16_bf16 v[16:31], v[40:43], v[44:47], v[16:31]\n" \
    "ds_read_b64_tr_b16 v[40:41], %[wa] offset:10240\n" \
    "ds_read_b64_tr_b16 v[42:43], %[wa] offset:11264\n" \
    "ds_read_b128 v[44:47], %[oa] offset:5120\n" \
    "s_waitcnt lgkmcnt(9)\n" \
    "v_mfma_f32_32x32x16_bf16 v[16:31], v[48:51], v[52:55], v[16:31]\n" \
    "ds_read_b64_tr_b16 v[48:49], %[wa] offset:12288\n" \
    "ds_read_b64_tr_b16 v[50:51], %[wa] offset:13312\n" \
    "ds_read_b128 v[52:55], %[oa] offset:6144\n" \
    "s_waitcnt lgkmcnt(9)\n" \
    "v_mfma_f32_32x32x16_bf16 v[16:31], v[56:59], v[60:63], v[16:31]\n" \
    "ds_read_b64_tr_b16 v[56:57], %[wa] offset:14336\n" \
    "ds_read_b64_tr_b16 v[58:59], %[wa] offset:15360\n" \
    "ds_read_b128 v[60:63], %[oa] offset:7168\n" \
    "s_waitcnt lgkmcnt(9)\n" \
    "v_mfma_f32_32x32x16_bf16 v[16:31], v[32:35], v[36:39], v[16:31]\n" \
    "ds_read_b64_tr_b16 v[32:33], %[wa] offset:16384\n" \
    "ds_read_b64_tr_b16 v[34:35], %[wa] offset:17408\n" \
    "ds_read_b128 v[36:39], %[oa] offset:8192\n" \
    "s_waitcnt lgkmcnt(9)\n" \
    "v_mfma_f32_32x32x16_bf16 v[16:31], v[40:43], v[44:47], v[16:31]\n" \
    "ds_read_b64_tr_b16 v[40:41], %[wa] offset:18432\n" \
    "ds_read_b64_tr_b16 v[42:43], %[wa] offset:19456\n" \
    "ds_read_b128 v[44:47], %[oa] offset:9216\n" \
    "s_waitcnt lgkmcnt(9)\n" \
    "v_mfma_f32_32x32x16_bf16 v[16:31], v[48:51], v[52:55], v[16:31]\n" \
    "ds_read_b64_tr_b16 v[48:49], %[wa] offset:20480\n" \
    "ds_read_b64_tr_b16 v[50:51], %[wa] offset:21504\n" \
    "ds_read_b128 v[52:55], %[oa] offset:10240\n" \
    "s_waitcnt lgkmcnt(9)\n" \
    "v_mfma_f32_32x32x16_bf16 v[16:31], v[56:59], v[60:63], v[16:31]\n" \
    "ds_read_b64_tr_b16 v[56:57], %[wa] offset:22528\n" \
    "ds_read_b64_tr_b16 v[58:59], %[wa] offset:23552\n" \
    "ds_read_b128 v[60:63], %[oa] offset:11264\n" \
    "s_waitcnt lgkmcnt(9)\n" \
    "v_mfma_f32_32x32x16_bf16 v[16:31], v[32:35], v[36:39], v[16:31]\n" \
    "ds_read_b64_tr_b16 v[32:33], %[wa] offset:24576\n" \
    "ds_read_b64_tr_b16 v[34:35], %[wa] offset:25600\n" \
    "ds_read_b128 v[36:39], %[oa] offset:12288\n" \
    "s_waitcnt lgkmcnt(9)\n" \
    "v_mfma_f32_32x32x16_bf16 v[16:31], v[40:43], v[44:47], v[16:31]\n" \
    "ds_read_b64_tr_b16 v[40:41], %[wa] offset:26624\n" \
    "ds_read_b64_tr_b16 v[42:43], %[wa] offset:27648\n" \
    "ds_read_b128 v[44:47], %[oa] offset:13312\n" \
    "s_waitcnt lgkmcnt(9)\n" \
    "v_mfma_f32_32x32x16_bf16 v[16:31], v[48:51], v[52:55], v[16:31]\n" \
    "ds_read_b64_tr_b16 v[48:49], %[wa] offset:28672\n" \
    "ds_read_b64_tr_b16 v[50:51], %[wa] offset:29696\n" \
    "ds_read_b128 v[52:55], %[oa] offset:14336\n" \
    "s_waitcnt lgkmcnt(9)\n" \
    "v_mfma_f32_32x32x16_bf16 v[16:31], v[56:59], v[60:63], v[16:31]\n" \
    "ds_read_b64_tr_b16 v[56:57], %[wa] offset:30720\n" \
    "ds_read_b64_tr_b16 v[58:59], %[wa] offset:31744\n" \
    "ds_read_b128 v[60:63], %[oa] offset:15360\n" \
    "s_waitcnt lgkmcnt(9)\n" \
    "v_mfma_f32_32x32x16_bf16 v[16:31], v[32:35], v[36:39], v[16:31]\n" \
    "s_waitcnt lgkmcnt(6)\n" \
    "v_mfma_f32_32x32x16_bf16 v[16:31], v[40:43], v[44:47], v[16:31]\n" \
    "s_waitcnt lgkmcnt(3)\n" \
    "v_mfma_f32_32x32x16_bf16 v[16:31], v[48:51], v[52:55], v[16:31]\n" \
    "s_waitcnt lgkmcnt(0)\n" \
    "v_mfma_f32_32x32x16_bf16 v[16:31], v[56:59], v[60:63], v[16:31]\n" \
    "s_nop 7\n" \
    "s_nop 3\n"

#define OPROJ_CLOBBERS "v32", "v33", "v34", "v35", "v36", "v37", "v38", "v39", "v40", "v41", "v42", "v43", "v44", "v45", "v46", "v47", "v48", "v49", "v50", "v51", "v52", "v53", "v54", "v55", "v56", "v57", "v58", "v59", "v60", "v61", "v62", "v63"


constexpr unsigned WS_EPOCH = 0;
constexpr unsigned WS_F0 = 4096;
constexpr unsigned WS_F1 = WS_F0 + 32768;
constexpr unsigned WS_F2 = WS_F1 + 32768;
constexpr unsigned WS_F3 = WS_F2 + 32768;
constexpr unsigned WS_Q = 262144;
constexpr unsigned WS_KV = WS_Q + 2097152;
constexpr unsigned WS_O = WS_KV + 4194304;
constexpr unsigned WS_UT = WS_O + 2097152;
constexpr unsigned WS_YT = WS_UT + 2097152;
constexpr unsigned WS_END = WS_YT + 2097152;

constexpr int SLOT = 32768;
constexpr int L_TAB = 3 * SLOT;
constexpr int L_KV = 155136;
constexpr int KV_CH = 2832;
constexpr int L_CNT = 163072;
constexpr int L_C1 = L_CNT + 40;
constexpr int L_CB = L_CNT + 44;
constexpr int L_C2 = L_CNT + 48;
constexpr int L_SCR = 163328;
constexpr int L_EP = 163584;
constexpr int L_U = 2 * SLOT;
constexpr int U_CH = 7680;
constexpr int L_ACOPY = L_U + 2 * U_CH;
constexpr int AC_CH = 16640;
constexpr int L_PART = L_ACOPY;
constexpr int L_WO = 0;
constexpr int L_OT = 65536;
constexpr int L_XP = 131072;
constexpr int L_QF = 139264;
constexpr int L_YT = 147456;
constexpr int LDS_BYTES = 163840;

constexpr float QSCALE = 0.25f * 1.4426950408889634f;

struct Params {
    const unsigned short* u; const unsigned short* lar; const unsigned short* aim; const unsigned short* cre; const unsigned short* cim;
    const unsigned short* ldt; const unsigned short* dsk; const unsigned short* x; const unsigned short* wqkv; const unsigned short* wo;
    unsigned short* out; char* ws;
};

__device__ __forceinline__ float bf2f(unsigned short b) { return __uint_as_float((unsigned)b << 16); }
__device__ __forceinline__ unsigned pkbf(float lo, float hi) { f32x2 v = {lo, hi}; return __builtin_bit_cast(unsigned, __builtin_convertvector(v, bf16x2)); }
__device__ __forceinline__ float rbf(float x) { return (float)(__bf16)x; }
__device__ __forceinline__ unsigned pkh(float lo, float hi) { f32x2 v = {lo, hi}; return __builtin_bit_cast(unsigned, __builtin_convertvector(v, f16x2)); }
__device__ __forceinline__ void glds16(unsigned voff, const void* sbase, unsigned ldsdst) {
    asm volatile("s_mov_b32 m0, %2\n\ts_nop 0\n\tglobal_load_lds_dwordx4 %0, %1" :: "v"(voff), "s"(sbase), "s"(ldsdst) : "memory");
}
__device__ __forceinline__ void glds16_dev(unsigned voff, const void* sbase, unsigned ldsdst) {
    asm volatile("s_mov_b32 m0, %2\n\ts_nop 0\n\tglobal_load_lds_dwordx4 %0, %1 sc1" :: "v"(voff), "s"(sbase), "s"(ldsdst) : "memory");
}
#define WAIT_VM(n) asm volatile("s_waitcnt vmcnt(%0)" :: "i"(n) : "memory")
#define BAR() asm volatile("s_waitcnt lgkmcnt(0)\n\ts_barrier" ::: "memory")
#define MFMA32(a, b, c) __builtin_amdgcn_mfma_f32_32x32x16_bf16(a, b, c, 0, 0, 0)
__device__ __forceinline__ bf16x8 lds_frag(const LAS char* p) { return *(const LAS bf16x8*)p; }
__device__ __forceinline__ s16x4 tr8(const LAS char* p) { return __builtin_amdgcn_ds_read_tr16_b64_v4i16((LAS s16x4*)p); }
__device__ __forceinline__ float exp_acc(float x) {
    const float t = x * 1.4426950216293335f;
    const float r = __builtin_fmaf(x, 1.4426950216293335f, -t) + x * 1.9259629911266175e-8f;
    const float e = __builtin_amdgcn_exp2f(t);
    return __builtin_fmaf(e, r * 0.6931471805599453f, e);
}
__device__ __forceinline__ void poll_flags(const char* ws, unsigned off, unsigned long long tag) {
    const gu64* f = (const gu64*)(ws + off);
#pragma nounroll
    for (unsigned spins = 0; spins < (1u << 16); ++spins) {
        if (__all(__hip_atomic_load(f, __ATOMIC_RELAXED, __HIP_MEMORY_SCOPE_AGENT) == tag)) break;
        __builtin_amdgcn_s_sleep(1);
    }
}
__device__ __forceinline__ void set_flag(char* ws, unsigned off, unsigned long long tag) {
    __hip_atomic_store((gu64*)(ws + off), tag, __ATOMIC_RELAXED, __HIP_MEMORY_SCOPE_AGENT);
}
__device__ __forceinline__ unsigned cnt_add(LAS char* L, int off, int lane) {
    asm volatile("s_waitcnt lgkmcnt(0)" ::: "memory");
    unsigned old = 0;
    if (lane == 0) old = __hip_atomic_fetch_add((LAS unsigned*)(L + off), 1u, __ATOMIC_RELAXED, __HIP_MEMORY_SCOPE_WORKGROUP);
    asm volatile("" ::: "memory");
    return old;
}
__device__ __forceinline__ void cnt_wait(LAS char* L, int off, unsigned target) {
#pragma nounroll
    for (unsigned spins = 0; spins < (1u << 18); ++spins) {
        if (__builtin_amdgcn_readfirstlane(*(volatile LAS unsigned*)(L + off)) >= target) break;
        __builtin_amdgcn_s_sleep(1);
    }
    asm volatile("" ::: "memory");
}
__device__ __forceinline__ void touch(unsigned voff, const void* sbase, unsigned lds0) {
    asm volatile("s_mov_b32 m0, %2\n\ts_nop 0\n\tglobal_load_lds_dword %0, %1" :: "v"(voff), "s"(sbase), "s"(lds0 + L_SCR) : "memory");
}

__device__ __forceinline__ void qkv_stage(const Params& p, unsigned lds0, int sw, int lane, int xcd, int idx, int s, int slot) {
    const int tt = 8 * (xcd >> 1) + (idx >> 2), g4 = 4 * (xcd & 1) + (idx & 3);
    const int sr = (s + (idx >> 2) + 2 * (idx & 3)) & 7;
    const char* xb = (const char*)p.x + (size_t)tt * 65536 + sr * 128;
    const char* wb = (const char*)p.wqkv + g4 * 128 + sr * 196608;
    const int lr = lane >> 3, lc = lane & 7;
    const unsigned vx = (16 * sw + lr) * 1024 + ((lc ^ (lr >> 1)) << 4);
    const unsigned vx2 = (16 * sw + 8 + lr) * 1024 + ((lc ^ (4 | (lr >> 1))) << 4);
    const unsigned vw = lr * 3072 + ((lc ^ (((lr >> 1) & 1) << 2)) << 4);
    const unsigned d = lds0 + slot * SLOT;
    glds16(vx, xb, d + sw * 2048); glds16(vx2, xb, d + sw * 2048 + 1024);
#pragma unroll
    for (int e = 0; e < 6; ++e) { const int pc = 6 * sw + e; glds16(vw, wb + (pc >> 3) * 1024 + (pc & 7) * 24576, d + 8192 + pc * 1024); }
}
__device__ __forceinline__ void phase_qkv(const Params& p, LAS char* L, unsigned lds0, int w, int lane, int xcd, int idx) {
    __builtin_amdgcn_s_setprio(2);
    const int tb = w & 1, cbh = w >> 1;
    const int r = lane & 31, h = lane >> 5;
    const LAS char* xa[4];
#pragma unroll
    for (int ks = 0; ks < 4; ++ks) xa[ks] = L + (32 * tb + r) * 128 + ((((2 * ks + h) ^ ((r >> 1) & 7))) << 4);
    const int q = (lane & 15) >> 2, pp = lane & 3;
    const LAS char* wa = L + 8192 + (8 * h + q) * 128 + (((cbh * 4 + ((lane >> 4) & 1) * 2 + (pp >> 1)) ^ ((q >> 1) << 2)) << 4) + (pp & 1) * 8;
    f32x16 aq = {0}, ak = {0}, av = {0};
    const unsigned xo0 = (unsigned)(uintptr_t)xa[0], xo1 = (unsigned)(uintptr_t)xa[1], xo2 = (unsigned)(uintptr_t)xa[2], xo3 = (unsigned)(uintptr_t)xa[3], wo_ = (unsigned)(uintptr_t)wa;
#define Q_STEP(s) do { \
        BAR(); \
        const unsigned so = ((s) % 3) * SLOT; \
        asm volatile(QKV_STEP : "+{v[16:31]}"(aq), "+{v[32:47]}"(ak), "+{v[48:63]}"(av) \
                     : [x0] "v"(xo0 + so), [x1] "v"(xo1 + so), [x2] "v"(xo2 + so), [x3] "v"(xo3 + so), [wa] "v"(wo_ + so) : "memory", QKV_CLOBBERS); } while (0)
    Q_STEP(0); Q_STEP(1); Q_STEP(2); Q_STEP(3); Q_STEP(4); Q_STEP(5); Q_STEP(6); Q_STEP(7);
#undef Q_STEP
    asm volatile("s_nop 7\n\ts_nop 3" : "+v"(aq), "+v"(ak), "+v"(av));
    __builtin_amdgcn_s_setprio(0);
    const int tt = 8 * (xcd >> 1) + (idx >> 2), g4 = 4 * (xcd & 1) + (idx & 3);
    const __amdgpu_buffer_rsrc_t rs = __builtin_amdgcn_make_buffer_rsrc(p.ws, 0, WS_END, 0x00020000);
    const int tbg = 2 * tt + tb, hb = 4 * g4 + 2 * cbh;
#pragma unroll
    for (int s = 0; s < 2; ++s) {
        u32x4 qv, kv, vv;
#pragma unroll
        for (int j = 0; j < 4; ++j) {
            qv[j] = pkbf(aq[8 * s + 2 * j] * QSCALE, aq[8 * s + 2 * j + 1] * QSCALE);
            kv[j] = pkbf(ak[8 * s + 2 * j], ak[8 * s + 2 * j + 1]);
            vv[j] = pkbf(av[8 * s + 2 * j], av[8 * s + 2 * j + 1]);
        }
        __builtin_amdgcn_raw_buffer_store_b128(kv, rs, WS_KV + (hb + s) * 131072 + tbg * 2048 + lane * 16, 0, 16);
        __builtin_amdgcn_raw_buffer_store_b128(vv, rs, WS_KV + (hb + ((lane >> 4) & 1)) * 131072 + tbg * 2048 + 1024 + h * 512 + s * 256 + (lane & 15) * 16, 0, 16);
        __builtin_amdgcn_raw_buffer_store_b128(qv, rs, WS_Q + ((hb + s) * 64 + tbg) * 1024 + lane * 16, 0, 16);
    }
}

struct cpx { float x, y; };
#define NOPK(v) asm volatile("" : "+v"(v))
constexpr int L_PRM = L_TAB + 8192;
constexpr int L_GP = L_TAB + 12288;
constexpr int L_G2K = L_TAB + 28672;
__device__ __forceinline__ cpx cexp_s(float a, float b) {
    const float m = __builtin_amdgcn_exp2f(a), f = __builtin_amdgcn_fractf(b);
    float re = m * __builtin_amdgcn_cosf(f); NOPK(re);
    float im = m * __builtin_amdgcn_sinf(f); NOPK(im);
    return (cpx){re, im};
}
template <int CTRL, int RM> __device__ __forceinline__ float dpp_f(float v) { return __int_as_float(__builtin_amdgcn_update_dpp(0, __float_as_int(v), CTRL, RM, 0xf, false)); }
__device__ __forceinline__ void gen_prep(LAS char* L, int v, int lane, unsigned short ldt, const unsigned short (&pr)[4], float (&sc_dn)[2]) {
    const float dt = rbf(exp_acc(bf2f(ldt)));
    const float ar = -rbf(exp_acc(bf2f(pr[0])));
    float zr = ar * dt; NOPK(zr); float zi = bf2f(pr[1]) * dt; NOPK(zi);
    float za = zr * 1.4426950408889634f; NOPK(za); float zb = zi * 0.15915494309189535f; NOPK(zb);
    float cr = bf2f(pr[2]) * dt; NOPK(cr); float ci = bf2f(pr[3]) * dt; NOPK(ci);
    float mx = fmaxf(fabsf(cr), fabsf(ci));
    mx = fmaxf(mx, dpp_f<0x111, 0xf>(mx)); mx = fmaxf(mx, dpp_f<0x112, 0xf>(mx)); mx = fmaxf(mx, dpp_f<0x114, 0xf>(mx)); mx = fmaxf(mx, dpp_f<0x118, 0xf>(mx));
    mx = fmaxf(mx, dpp_f<0x142, 0xa>(mx));
    float up[2];
#pragma unroll
    for (int ch = 0; ch < 2; ++ch) {
        int ex = (int)((__builtin_amdgcn_readlane(__float_as_int(mx), 31 + 32 * ch) >> 23) & 255) - 126;
        ex = ex < -100 ? -100 : (ex > 100 ? 100 : ex);
        up[ch] = __uint_as_float((unsigned)(128 - ex) << 23);
        sc_dn[ch] = __uint_as_float((unsigned)(127 + ex) << 23);
    }
    const float u = lane < 32 ? up[0] : up[1];
    float xr = cr * u; NOPK(xr); float xi = ci * u; NOPK(xi);
    *(LAS f32x4*)(L + L_PRM + v * 1024 + lane * 16) = (f32x4){za, zb, xr, xi};
    const cpx e = cexp_s(za * 2048.f, zb * 2048.f);
    float k2 = xr * e.x; NOPK(k2); k2 = __builtin_fmaf(-xi, e.y, k2);
    k2 += dpp_f<0x111, 0xf>(k2); k2 += dpp_f<0x112, 0xf>(k2); k2 += dpp_f<0x114, 0xf>(k2); k2 += dpp_f<0x118, 0xf>(k2); k2 += dpp_f<0x142, 0xa>(k2);
    if ((lane & 31) == 31) *(LAS float*)(L + L_G2K + (lane >> 5) * 16 + v * 4) = k2 * (lane < 32 ? sc_dn[0] : sc_dn[1]);
}
struct GenFr { u32x4 a[4], b[4]; f32x4 acc[4]; };
template <int E, int JJ>
__device__ __forceinline__ void gen_pair(GenFr& f, const f32x4& q0, const f32x4& q1, float r) {
    float a0 = q0[0] * r; NOPK(a0); float b0 = q0[1] * r; NOPK(b0); float a1 = q1[0] * r; NOPK(a1); float b1 = q1[1] * r; NOPK(b1);
    cpx w0 = cexp_s(a0, b0), w1 = cexp_s(a1, b1);
    float t0, t1, t2, t3;
    unsigned B0[4], A0[4], B1[4], A1[4];
    asm volatile(GEN_PAIR
        : "=&v"(B0[0]), "=&v"(B0[1]), "=&v"(B0[2]), "=&v"(B0[3]), "=&v"(A0[0]), "=&v"(A0[1]), "=&v"(A0[2]), "=&v"(A0[3]),
          "=&v"(B1[0]), "=&v"(B1[1]), "=&v"(B1[2]), "=&v"(B1[3]), "=&v"(A1[0]), "=&v"(A1[1]), "=&v"(A1[2]), "=&v"(A1[3]),
          "+v"(w0.x), "+v"(w0.y), "+v"(w1.x), "+v"(w1.y), "=&v"(t0), "=&v"(t1), "=&v"(t2), "=&v"(t3)
        : "v"(q0[2]), "v"(q0[3]), "v"(q1[2]), "v"(q1[3]));
#pragma unroll
    for (int t = 0; t < 4; ++t) { f.b[t][JJ] = B0[t]; f.b[t][JJ + 1] = B1[t]; f.a[t][JJ] = A0[t]; f.a[t][JJ + 1] = A1[t]; }
    if (JJ == 2) {
        __builtin_amdgcn_s_setprio(3);
#pragma unroll
        for (int t = 0; t < 4; ++t) {
            if (E == 0) f.acc[t] = (f32x4){0, 0, 0, 0};
            f.acc[t] = __builtin_amdgcn_mfma_f32_16x16x32_f16(__builtin_bit_cast(f16x8, f.a[t]), __builtin_bit_cast(f16x8, f.b[t]), f.acc[t], 0, 0, 0);
        }
        __builtin_amdgcn_s_setprio(0);
    }
}
__device__ __forceinline__ void gen_partials(const GenFr& f, LAS char* L, int v, int lane, float dn) {
#pragma unroll
    for (int t = 0; t < 4; ++t) {
        float o0 = f.acc[t][0] * dn; NOPK(o0); float o1 = f.acc[t][1] * dn; NOPK(o1); float o2 = f.acc[t][2] * dn; NOPK(o2); float o3 = f.acc[t][3] * dn; NOPK(o3);
        *(LAS f32x4*)(L + L_GP + (v * 4 + t) * 1024 + lane * 16) = (f32x4){o0, o1, o2, o3};
    }
}
struct GenRd { f32x4 s[4], k2; };
__device__ __forceinline__ void gen_reduce_load(GenRd& r, LAS char* L, int t, int ch, int lane) {
    const LAS char* P = L + L_GP + t * 1024 + lane * 16;
#pragma unroll
    for (int i = 0; i < 4; ++i) r.s[i] = *(const LAS f32x4*)(P + i * 4096);
    r.k2 = *(const LAS f32x4*)(L + L_G2K + ch * 16);
}
__device__ __forceinline__ void gen_reduce_store(const GenRd& r, LAS char* L, int t, int ch, int lane) {
    LAS unsigned short* KV = (LAS unsigned short*)(L + L_KV + ch * KV_CH);
    const int rc = lane & 15, gq = lane >> 4;
#pragma unroll
    for (int i = 0; i < 4; ++i) {
        const int row = 4 * gq + i, pos = (16 * row + rc) << t;
        float sum = r.s[0][i] + r.s[1][i]; NOPK(sum); float s23 = r.s[2][i] + r.s[3][i]; NOPK(s23); sum += s23;
        const unsigned pk = pkbf(sum, (float)pos);
        const int ix = pos == 0 ? 1409 : (int)(pk >> 16) - 16256;
        if (t == 0 || row >= 8) KV[ix] = (unsigned short)(pk & 0xffffu);
    }
    if (t == 3 && lane == 0) KV[17664 - 16256] = (unsigned short)(pkbf((r.k2[0] + r.k2[1]) + (r.k2[2] + r.k2[3]), 0.f) & 0xffffu);
}
template <bool LAST>
__device__ __forceinline__ void conv_expand_w(LAS char* L, int tid) {
    const int ch = tid >> 8, cidx = tid & 255;
    const unsigned kb = L_KV + ch * KV_CH - 2 * 16256;
    const float f0 = (float)(2047 - 8 * cidx);
    unsigned pe[6];
#pragma unroll
    for (int k = 0; k < 12; k += 2) {
        float a = f0 - (float)k, b = f0 - (float)(k + 1);
        if (LAST) { a = fmaxf(a, 1.0f); b = fmaxf(b, 1.0f); }
        const unsigned bits = pkbf(a, b);
        typedef unsigned short u16x2 __attribute__((ext_vector_type(2)));
        u16x2 v;
        v[0] = *(const LAS unsigned short*)(L + kb + 2 * (bits & 0xffffu));
        v[1] = k + 1 < 11 ? *(const LAS unsigned short*)(L + kb + 2 * (bits >> 16)) : (unsigned short)0;
        pe[k >> 1] = __builtin_bit_cast(unsigned, v);
    }
    if (LAST && cidx == 255) {
        pe[3] = (pe[3] & 0xffffu) | ((unsigned)*(const LAS unsigned short*)(L + L_KV + ch * KV_CH + 2 * 1409) << 16); pe[4] = 0; pe[5] = 0;
    }
    unsigned po[5];
#pragma unroll
    for (int k = 0; k < 5; ++k) po[k] = __builtin_amdgcn_alignbyte(pe[k + 1], pe[k], 2);
    LAS char* A = L + L_ACOPY + ch * AC_CH + cidx * 16;
    *(LAS u32x4*)(A) = (u32x4){pe[0], pe[1], pe[2], pe[3]};
    *(LAS u32x4*)(A + 4160) = (u32x4){po[0], po[1], po[2], po[3]};
    *(LAS u32x4*)(A + 2 * 4160) = (u32x4){pe[1], pe[2], pe[3], pe[4]};
    *(LAS u32x4*)(A + 3 * 4160) = (u32x4){po[1], po[2], po[3], po[4]};
    if (LAST && cidx >= 240) *(LAS u32x4*)(L + L_ACOPY + ch * AC_CH + ((cidx - 240) >> 2) * 4160 + 4096 + (cidx & 3) * 16) = (u32x4){0, 0, 0, 0};
}
__device__ __forceinline__ void conv_expand(LAS char* L, int w, int tid) {
    if ((w & 3) == 3) conv_expand_w<true>(L, tid); else conv_expand_w<false>(L, tid);
}
__device__ __forceinline__ void phase_gen(const Params& p, LAS char* L, int v, int lane, int xcd, int idx) {
    const int d = 64 * xcd + 2 * idx + (lane >> 5), o = d * 128 + 32 * v + (lane & 31);
    const unsigned short ldt = p.ldt[d];
    const unsigned short pr[4] = {p.lar[o], p.aim[o], p.cre[o], p.cim[o]};
    GenFr f; float dn[2];
    const float r = (float)(lane & 15);
    f32x4 q0[8], q1[8]; GenRd rd;
#define PRM_LOAD(q, ch) _Pragma("unroll") for (int k = 0; k < 8; ++k) q[k] = *(const LAS f32x4*)(L + L_PRM + v * 1024 + (32 * (ch) + 16 * (k >> 2) + 4 * (lane >> 4) + (k & 3)) * 16)
    gen_prep(L, v, lane, ldt, pr, dn);
    asm volatile("s_waitcnt lgkmcnt(0)" ::: "memory");
    PRM_LOAD(q0, 0); PRM_LOAD(q1, 1);
    gen_pair<0, 0>(f, q0[0], q0[1], r); gen_pair<0, 2>(f, q0[2], q0[3], r);
    BAR();
    const __amdgpu_buffer_rsrc_t rs = __builtin_amdgcn_make_buffer_rsrc(p.ws, 0, WS_END, 0x00020000);
    {
        const int g = lane >> 4, q = (lane & 15) >> 2, pp = lane & 3;
        const LAS char* a = L + L_TAB + (8 * g + q) * 128 + v * 32 + pp * 8;
#pragma unroll
        for (int e = 0; e < 2; ++e) {
            const s16x8 tv = __builtin_shufflevector(tr8(a + e * 4096), tr8(a + e * 4096 + 512), 0, 1, 2, 3, 4, 5, 6, 7);
            __builtin_amdgcn_raw_buffer_store_b128(__builtin_bit_cast(u32x4, tv), rs, WS_UT + (64 * xcd + 16 * v + (lane & 15)) * 4096 + idx * 128 + (g + 4 * e) * 16, 0, 16);
        }
    }
    gen_pair<1, 0>(f, q0[4], q0[5], r);
    BAR();
    gen_pair<1, 2>(f, q0[6], q0[7], r);
    gen_partials(f, L, v, lane, dn[0]);
    WAIT_VM(0);
    BAR();
    const unsigned long long tag = *(const LAS unsigned long long*)(L + L_EP) + 1ull;
    if (v == 0 && lane == 0) set_flag(p.ws, WS_F0 + blockIdx.x * 128, tag);
    gen_reduce_load(rd, L, v, 0, lane);
    __builtin_amdgcn_sched_barrier(0);
    gen_pair<0, 0>(f, q1[0], q1[1], r);
    __builtin_amdgcn_sched_barrier(0);
    gen_reduce_store(rd, L, v, 0, lane);
    BAR();
    gen_pair<0, 2>(f, q1[2], q1[3], r);
    BAR();
    gen_pair<1, 0>(f, q1[4], q1[5], r);
    BAR();
    const unsigned fo = WS_F0 + (xcd + 8 * (lane & 31)) * 128;
    unsigned long long f0 = 0;
    if (v == 0) f0 = __hip_atomic_load((const gu64*)(p.ws + fo), __ATOMIC_RELAXED, __HIP_MEMORY_SCOPE_AGENT);
    gen_pair<1, 2>(f, q1[6], q1[7], r);
    gen_partials(f, L, v, lane, dn[1]);
    if (v == 0 && !__all(f0 == tag)) poll_flags(p.ws, fo, tag);
    BAR();
    gen_reduce_load(rd, L, v, 1, lane);
    gen_reduce_store(rd, L, v, 1, lane);
    BAR();
    conv_expand(L, v, v * 64 + lane); conv_expand(L, v, 256 + v * 64 + lane);
#undef PRM_LOAD
}

__device__ __forceinline__ void conv_mfma(unsigned lds0, int w, int lane, f32x16& y0, f32x16& y1) {
    const int ch = w >> 2, role = w & 3, r = lane & 31, h = lane >> 5;
    const int dl = role == 0 ? 11 : (role == 1 ? 23 : (role == 2 ? 39 : 63));
    const unsigned aP = lds0 + L_ACOPY + ch * AC_CH + (3 - (r & 3)) * 4160 + 2 * (2044 - 4 * (r >> 2) + 8 * h) - 64 * dl;
    const unsigned bP = lds0 + L_U + ch * U_CH + (32 + r) * 80 + 16 * h - 80 * dl;
#define ZV(i) "v_mov_b32 v" #i ", 0\n"
#define CONV_ASM(BODY) asm volatile( \
        "v_mov_b32 v48, %[a]\n v_add_u32 v49, 8, %[a]\n v_mov_b32 v50, %[b]\n" \
        ZV(16) ZV(17) ZV(18) ZV(19) ZV(20) ZV(21) ZV(22) ZV(23) ZV(24) ZV(25) ZV(26) ZV(27) ZV(28) ZV(29) ZV(30) ZV(31) \
        ZV(32) ZV(33) ZV(34) ZV(35) ZV(36) ZV(37) ZV(38) ZV(39) ZV(40) ZV(41) ZV(42) ZV(43) ZV(44) ZV(45) ZV(46) ZV(47) \
        BODY "s_nop 7\n s_nop 7\n" \
        : "=&{v[16:31]}"(y0), "=&{v[32:47]}"(y1) : [a] "v"(aP), [b] "v"(bP) : "memory", "v48", "v49", "v50", CONV_CLOBBERS)
    if (role < 2) { CONV_ASM(CONV_R0); } else if (role == 2) { CONV_ASM(CONV_R2); } else { CONV_ASM(CONV_R3); }
#undef CONV_ASM
#undef ZV
}
__device__ __forceinline__ void conv_partials(LAS char* L, int w, int lane, const f32x16& y0, const f32x16& y1) {
    const int ch = w >> 2, role = w & 3;
    LAS char* P0 = L + L_PART + ch * 28672 + lane * 16;
#pragma unroll
    for (int q = 0; q < 4; ++q) {
        if (role < 3) *(LAS f32x4*)(P0 + role * 4096 + q * 1024) = (f32x4){y0[4 * q], y0[4 * q + 1], y0[4 * q + 2], y0[4 * q + 3]};
        *(LAS f32x4*)(P0 + (3 + role) * 4096 + q * 1024) = (f32x4){y1[4 * q], y1[4 * q + 1], y1[4 * q + 2], y1[4 * q + 3]};
    }
}
__device__ __forceinline__ void conv_final(const Params& p, LAS char* L, int w, int lane, int xcd, int idx) {
    const int ch = w >> 2, T = (w >> 1) & 1, i = 16 * (w & 1) + (lane & 15), q = lane >> 4;
    const LAS char* P0 = L + L_PART + ch * 28672 + (T ? 3 * 4096 : 0) + q * 1024 + i * 16;
    f32x4 s0 = *(const LAS f32x4*)P0 + *(const LAS f32x4*)(P0 + 4096) + *(const LAS f32x4*)(P0 + 8192);
    f32x4 s1 = *(const LAS f32x4*)(P0 + 512) + *(const LAS f32x4*)(P0 + 4096 + 512) + *(const LAS f32x4*)(P0 + 8192 + 512);
    if (T) { s0 += *(const LAS f32x4*)(P0 + 12288); s1 += *(const LAS f32x4*)(P0 + 12288 + 512); }
    const u32x4 v = {pkbf(s0[0], s0[1]), pkbf(s0[2], s0[3]), pkbf(s1[0], s1[1]), pkbf(s1[2], s1[3])};
    const __amdgpu_buffer_rsrc_t rs = __builtin_amdgcn_make_buffer_rsrc(p.ws, 0, WS_END, 0x00020000);
    __builtin_amdgcn_raw_buffer_store_b128(v, rs, WS_YT + (64 * xcd + 2 * idx + ch) * 4096 + (32 * (32 * T + i) + 8 * q) * 2, 0, 16);
}

__device__ __forceinline__ void att_fallback(LAS char* L, int lane, bf16x8 q, f32x16& O) {
    const LAS char* base = L + lane * 16;
    float m = -INFINITY;
    for (int kb = 0; kb < 64; ++kb) {
        f32x16 s = {0};
        s = MFMA32(lds_frag(base + kb * 2048), q, s);
#pragma unroll
        for (int i = 0; i < 16; ++i) m = fmaxf(m, s[i]);
    }
    m = fmaxf(m, __shfl_xor(m, 32));
    f32x16 c;
#pragma unroll
    for (int i = 0; i < 16; ++i) c[i] = -m;
    f32x16 o = {0};
    const bool real = (lane & 31) < 16;
    const LAS char* vb = L + 1024 + (lane >> 5) * 512 + (lane & 15) * 16;
    const unsigned pad = (lane & 31) == 16 ? 0x3F803F80u : 0u;
    const u32x4 ones = {pad, pad, pad, pad};
    for (int kb = 0; kb < 64; ++kb) {
        const f32x16 s = MFMA32(lds_frag(base + kb * 2048), q, c);
        u32x4 p0, p1;
#pragma unroll
        for (int j = 0; j < 4; ++j) {
            p0[j] = pkbf(__builtin_amdgcn_exp2f(s[2 * j]), __builtin_amdgcn_exp2f(s[2 * j + 1]));
            p1[j] = pkbf(__builtin_amdgcn_exp2f(s[8 + 2 * j]), __builtin_amdgcn_exp2f(s[8 + 2 * j + 1]));
        }
        u32x4 v0 = *(const LAS u32x4*)(vb + kb * 2048), v1 = *(const LAS u32x4*)(vb + kb * 2048 + 256);
        if (!real) { v0 = ones; v1 = ones; }
        o = MFMA32(__builtin_bit_cast(bf16x8, v0), __builtin_bit_cast(bf16x8, p0), o);
        o = MFMA32(__builtin_bit_cast(bf16x8, v1), __builtin_bit_cast(bf16x8, p1), o);
    }
    O = o;
}
__device__ __forceinline__ void phase_att(const Params& p, LAS char* L, unsigned lds0, int w, int lane, int xcd, int idx) {
    const int head = 4 * xcd + (idx >> 3), qt = idx & 7, tbq = 8 * qt + w;
    const unsigned rd = lds0 + lane * 16, rq = lds0 + L_QF + w * 1024 + lane * 16;
    f32x16 O; u32x4 qf;
    asm volatile(
        "ds_read_b128 v[96:99], %[rq]\n"
        "ds_read_b128 v[72:75], %[rd]\n"
        "s_mov_b32 s42, 0x0000ffff\n s_mov_b32 s43, 0x0000ffff\n"
        "v_mov_b32 v116, %[rd]\n"
#define Z(i) "v_mov_b32 v" #i ", 0\n"
        Z(80) Z(81) Z(82) Z(83) Z(84) Z(85) Z(86) Z(87) Z(88) Z(89) Z(90) Z(91) Z(92) Z(93) Z(94) Z(95)
        "s_mov_b32 s44, 0x00010000\n s_mov_b32 s45, 0x00010000\n s_mov_b64 exec, s[44:45]\n"
#define ONE(i) "v_mov_b32 v" #i ", 0x3f803f80\n"
        ONE(80) ONE(81) ONE(82) ONE(83) ONE(84) ONE(85) ONE(86) ONE(87) ONE(88) ONE(89) ONE(90) ONE(91) ONE(92) ONE(93) ONE(94) ONE(95)
#undef ONE
        "s_mov_b64 exec, s[42:43]\n"
        "ds_read_b128 v[80:83], v116 offset:1024\n ds_read_b128 v[84:87], v116 offset:1280\n"
        "s_mov_b64 exec, -1\n"
        "s_waitcnt lgkmcnt(2)\n"
        "v_mfma_f32_32x32x16_bf16 v[16:31], v[72:75], v[96:99], 0\n"
        Z(48) Z(49) Z(50) Z(51) Z(52) Z(53) Z(54) Z(55) Z(56) Z(57) Z(58) Z(59) Z(60) Z(61) Z(62) Z(63)
#undef Z
        "v_max3_f32 v100, v16, v17, v18\n v_max3_f32 v101, v19, v20, v21\n v_max3_f32 v102, v22, v23, v24\n v_max3_f32 v103, v25, v26, v27\n"
        "v_max3_f32 v100, v100, v28, v29\n v_max3_f32 v101, v101, v30, v31\n v_max3_f32 v100, v100, v102, v103\n v_max_f32 v100, v100, v101\n"
        "v_mov_b32 v101, v100\n v_mov_b32 v68, 0\n v_mov_b32 v69, 0\n v_permlane32_swap_b32 v100, v101\n v_mov_b32 v70, 0\n v_mov_b32 v71, 0\n"
        "v_max_f32 v100, v100, v101\n v_mov_b32 v134, 0\n v_mov_b32 v135, 0\n v_sub_f32 v100, 0, v100\n"
#define CM(i) "v_mov_b32 v" #i ", v100\n"
        CM(101) CM(102) CM(103) CM(104) CM(105) CM(106) CM(107) CM(108) CM(109) CM(110) CM(111) CM(112) CM(113) CM(114) CM(115)
#undef CM
        "s_nop 1\n"
        "v_mfma_f32_32x32x16_bf16 v[16:31], v[72:75], v[96:99], v[100:115]\n"
        "ds_read_b128 v[72:75], v116 offset:2048\n"
        "s_mov_b32 s44, 2\n s_mov_b32 s45, 0\n s_mov_b32 s46, 0\n"
        "s_nop 7\n s_nop 1\n"
        "L_att_loop_%=:\n"
        "s_cmp_lt_u32 s46, 4\n s_cbranch_scc1 L_att_p2_%=\n s_cmp_lt_u32 s46, 7\n s_cbranch_scc1 L_att_p1_%=\n"
        "s_setprio 0\n s_branch L_att_go_%=\n L_att_p2_%=:\n s_setprio 2\n s_branch L_att_go_%=\n L_att_p1_%=:\n s_setprio 1\n L_att_go_%=:\n"
        "s_add_u32 s46, s46, 1\n"
        ATT_BODY8
        "v_add_u32 v116, 0x4000, v116\n"
        "s_sub_u32 s44, s44, 1\n s_cmp_lg_u32 s44, 0\n s_cbranch_scc1 L_att_loop_%=\n"
        "s_cmp_lg_u32 s45, 0\n s_cbranch_scc1 L_att_done_%=\n"
        "s_mov_b32 s45, 1\n"
        "s_waitcnt vmcnt(0)\n s_barrier\n"
        "s_mov_b32 s44, 6\n s_branch L_att_loop_%=\n"
        "L_att_done_%=:\n"
        "s_setprio 0\n"
        "v_cvt_pk_bf16_f32 v71, v134, v135\n s_nop 1\n"
        "v_mfma_f32_32x32x16_bf16 v[48:63], v[92:95], v[68:71], v[48:63]\n"
        "s_waitcnt lgkmcnt(0)\n s_nop 7\n s_nop 7\n"
        : "=&{v[48:63]}"(O), "=&{v[96:99]}"(qf)
        : [rq] "v"(rq), [rd] "v"(rd)
        : "memory", "scc", "s42", "s43", "s44", "s45", "s46",
          ATT_CLOBBERS);
    float l = __shfl(O[8], lane & 31);
    bool bad = !(l <= 1.152921504606847e18f);
#pragma unroll
    for (int j = 0; j < 8; ++j) bad = bad || !(fabsf(O[j]) <= 3.0e38f);
    if (__any(bad)) { att_fallback(L, lane, __builtin_bit_cast(bf16x8, qf), O); l = __shfl(O[8], lane & 31); }
    const float rl = 1.0f / l;
    u32x4 ov;
#pragma unroll
    for (int j = 0; j < 4; ++j) ov[j] = pkbf(O[2 * j] * rl, O[2 * j + 1] * rl);
    const __amdgpu_buffer_rsrc_t rs = __builtin_amdgcn_make_buffer_rsrc(p.ws, 0, WS_END, 0x00020000);
    __builtin_amdgcn_raw_buffer_store_b128(ov, rs, WS_O + (tbq * 32 + head) * 1024 + lane * 16, 0, 16);
}

__device__ __forceinline__ void oproj_stage_w(const Params& p, unsigned lds0, int w, int lane, int idx) {
    const char* wb = (const char*)p.wo + (idx & 7) * 128;
    const int lr = lane >> 3, lc = lane & 7;
    const unsigned vo = lr * 1024 + ((lc ^ (((lr >> 1) & 1) << 2)) << 4);
#pragma unroll
    for (int e = 0; e < 8; ++e) glds16(vo, wb + (w * 8 + e) * 8192, lds0 + L_WO + (w * 8 + e) * 1024);
}
__device__ __forceinline__ f32x16 oproj_mfma(unsigned lds0, int w, int lane) {
    const int rt = w & 1, ct = (w >> 1) & 1, kh = w >> 2, h = lane >> 5;
    const int q = (lane & 15) >> 2, pp = lane & 3;
    const unsigned wa = lds0 + L_WO + (kh * 256 + 4 * h + q) * 128 + (((ct * 4 + ((lane >> 4) & 1) * 2 + (pp >> 1)) ^ ((q >> 1) << 2)) << 4) + (pp & 1) * 8;
    const unsigned oa = lds0 + L_OT + (rt * 32 + kh * 16) * 1024 + lane * 16;
    f32x16 acc;
    asm volatile(OPROJ_MFMA : "=&{v[16:31]}"(acc) : [wa] "v"(wa), [oa] "v"(oa) : "memory", OPROJ_CLOBBERS);
    return acc;
}
__device__ __forceinline__ void oproj_partial_out(LAS char* L, int w, int lane, const f32x16& acc) {
    LAS char* P0 = L + L_XP + (w & 3) * 4096 + lane * 16;
#pragma unroll
    for (int q = 0; q < 4; ++q) *(LAS f32x4*)(P0 + q * 1024) = (f32x4){acc[4 * q], acc[4 * q + 1], acc[4 * q + 2], acc[4 * q + 3]};
}
__device__ __forceinline__ void oproj_final(const Params& p, LAS char* L, int w, int lane, int xcd, int idx, const f32x16& acc) {
    const int tp = 4 * xcd + (idx >> 3), cg = idx & 7, rt = w & 1, ct = (w >> 1) & 1, h = lane >> 5;
    const LAS char* P0 = L + L_XP + (w & 3) * 4096 + lane * 16;
    u32x2 pk[4];
#pragma unroll
    for (int q = 0; q < 4; ++q) {
        const f32x4 o = *(const LAS f32x4*)(P0 + q * 1024);
        pk[q][0] = pkbf(acc[4 * q] + o[0], acc[4 * q + 1] + o[1]); pk[q][1] = pkbf(acc[4 * q + 2] + o[2], acc[4 * q + 3] + o[3]);
    }
    char* ob = (char*)p.out + (size_t)(64 * tp + 32 * rt + (lane & 31)) * 2048 + 1024 + cg * 128 + ct * 64 + h * 16;
#pragma unroll
    for (int q = 0; q < 4; q += 2) {
        const auto r0 = __builtin_amdgcn_permlane32_swap(pk[q][0], pk[q + 1][0], false, false);
        const auto r1 = __builtin_amdgcn_permlane32_swap(pk[q][1], pk[q + 1][1], false, false);
        *(u32x4*)(ob + q * 16) = (u32x4){r0[0], r1[0], r0[1], r1[1]};
    }
}

__device__ __forceinline__ void service(const Params& p, LAS char* L, unsigned lds0, int sw, int lane, int xcd, int idx) {
    const __amdgpu_buffer_rsrc_t rs = __builtin_amdgcn_make_buffer_rsrc(p.ws, 0, WS_END, 0x00020000);
    const int d0 = 64 * xcd + 2 * idx;
    qkv_stage(p, lds0, sw, lane, xcd, idx, 0, 0);
    {
        const char* ub = (const char*)p.u + (size_t)idx * 65536 + xcd * 128;
#pragma unroll
        for (int e = 0; e < 2; ++e) glds16((16 * sw + 8 * e + (lane >> 3)) * 1024 + (lane & 7) * 16, ub, lds0 + L_TAB + (16 * sw + 8 * e) * 128);
    }
    asm volatile("s_mov_b32 m0, %2\n\ts_nop 0\n\tglobal_load_lds_dword %0, %1 sc1" :: "v"((lane & 1) * 4), "s"(p.ws + WS_EPOCH), "s"(lds0 + L_EP) : "memory");
    qkv_stage(p, lds0, sw, lane, xcd, idx, 1, 1);
    WAIT_VM(8); BAR();
    qkv_stage(p, lds0, sw, lane, xcd, idx, 2, 2);
    const unsigned long long tag = *(const LAS unsigned long long*)(L + L_EP) + 1ull;
    WAIT_VM(8);
    BAR();
    qkv_stage(p, lds0, sw, lane, xcd, idx, 3, 0);
    WAIT_VM(8); BAR();
    qkv_stage(p, lds0, sw, lane, xcd, idx, 4, 1);
    WAIT_VM(8); BAR();
    qkv_stage(p, lds0, sw, lane, xcd, idx, 5, 2);
    WAIT_VM(8); BAR();
    qkv_stage(p, lds0, sw, lane, xcd, idx, 6, 0);
    WAIT_VM(8); BAR();
    qkv_stage(p, lds0, sw, lane, xcd, idx, 7, 1);
    WAIT_VM(8); BAR();
    {
        const char* utb = p.ws + WS_UT + (size_t)d0 * 4096;
#pragma unroll
        for (int e = 0; e < 3; ++e) {
            int k = sw + 4 * e; k = k > 9 ? k - 4 : k;
            const int ch = k >= 5 ? 1 : 0, kk = k - 5 * ch, id = kk * 64 + lane, row = id / 5, c5 = id - 5 * row;
            glds16_dev(ch * 4096 + row * 64 + (c5 > 3 ? 3 : c5) * 16, utb, lds0 + L_U + ch * U_CH + 2560 + kk * 1024);
        }
        const u32x4 z = {0, 0, 0, 0};
        const int k = sw * 64 + lane;
        if (k < 160) { *(LAS u32x4*)(L + L_U + k * 16) = z; *(LAS u32x4*)(L + L_U + U_CH + k * 16) = z; }
    }
    WAIT_VM(3); BAR();
    WAIT_VM(0); BAR();
    const int head = 4 * xcd + (idx >> 3), qt = idx & 7;
    const unsigned f1o = WS_F1 + (2 * ((lane & 31) >> 3) + (xcd >> 2) + 8 * (4 * (lane & 7) + (xcd & 3))) * 128;
    const char* kvb = p.ws + WS_KV + (size_t)head * 131072;
    const char* qb = p.ws + WS_Q + (size_t)(head * 64 + 8 * qt) * 1024;
    const int ttq = 4 * qt + sw;
    unsigned pend = 0xffffu | (1u << ttq);
    bool b10 = false;
#pragma nounroll
    for (unsigned spins = 0; spins < (1u << 16) && pend; ++spins) {
        unsigned rdy = (unsigned)__ballot(__hip_atomic_load((const gu64*)(p.ws + f1o), __ATOMIC_RELAXED, __HIP_MEMORY_SCOPE_AGENT) == tag) & pend;
        pend &= ~rdy;
        while (rdy) {
            const int tt = __builtin_ctz(rdy); rdy &= rdy - 1;
            if (tt < 16) glds16_dev(lane * 16, kvb + (4 * tt + sw) * 1024, lds0 + (4 * tt + sw) * 1024);
            if (tt == ttq) { glds16_dev(lane * 16, qb + 2 * sw * 1024, lds0 + L_QF + 2 * sw * 1024); glds16_dev(lane * 16, qb + (2 * sw + 1) * 1024, lds0 + L_QF + (2 * sw + 1) * 1024); }
        }
        if (pend && !b10 && __builtin_amdgcn_readfirstlane(*(volatile LAS unsigned*)(L + L_CB))) { BAR(); b10 = true; }
        if (pend) __builtin_amdgcn_s_sleep(1);
    }
    if (!b10) BAR();
    WAIT_VM(0); BAR();
    __builtin_amdgcn_s_setprio(3);
    conv_final(p, L, sw, lane, xcd, idx); conv_final(p, L, sw + 4, lane, xcd, idx);
    cnt_add(L, L_C2, lane); cnt_wait(L, L_C2, 4);
    pend = 0xffff0000u;
#pragma nounroll
    for (unsigned spins = 0; spins < (1u << 16) && pend; ++spins) {
        unsigned rdy = (unsigned)__ballot(__hip_atomic_load((const gu64*)(p.ws + f1o), __ATOMIC_RELAXED, __HIP_MEMORY_SCOPE_AGENT) == tag) & pend;
        pend &= ~rdy;
        while (rdy) { const int tt = __builtin_ctz(rdy); rdy &= rdy - 1; glds16_dev(lane * 16, kvb + (4 * tt + sw) * 1024, lds0 + (4 * tt + sw) * 1024); }
        if (pend) __builtin_amdgcn_s_sleep(1);
    }
    __builtin_amdgcn_s_setprio(0);
    WAIT_VM(0); BAR();
    if (sw == 0 && lane == 0) set_flag(p.ws, WS_F3 + blockIdx.x * 128, tag);
    if (sw < 2) touch((unsigned)(((idx * 2 + sw) * 64 + lane) * 128), (const char*)p.wo, lds0);
    const int tp = 4 * xcd + (idx >> 3), cg = idx & 7;
    poll_flags(p.ws, WS_F3 + (cg + 8 * (lane & 31)) * 128, tag);
    {
        const char* yb = p.ws + WS_YT + (size_t)cg * 262144 + tp * 128;
#pragma unroll
        for (int e = 0; e < 2; ++e) glds16_dev((16 * sw + 8 * e + (lane >> 3)) * 4096 + (lane & 7) * 16, yb, lds0 + L_YT + (16 * sw + 8 * e) * 128);
        u32x4 uv[2];
        const int tk = lane & 15, g = lane >> 4;
#pragma unroll
        for (int e = 0; e < 2; ++e) uv[e] = *(const u32x4*)((const char*)p.u + (size_t)(64 * tp + 16 * ((g >> 1) + 2 * e) + tk) * 1024 + cg * 128 + sw * 32 + (g & 1) * 16);
        const u32x4 dv = *(const u32x4*)((const char*)p.dsk + cg * 128 + sw * 32 + (g & 1) * 16);
        WAIT_VM(0);
#pragma unroll
        for (int e = 0; e < 2; ++e) {
            const LAS char* a = L + L_YT + (16 * sw + 8 * (g & 1) + (tk >> 2)) * 128 + 32 * ((g >> 1) + 2 * e) + (tk & 3) * 8;
            const u32x4 yv = __builtin_bit_cast(u32x4, __builtin_shufflevector(tr8(a), tr8(a + 512), 0, 1, 2, 3, 4, 5, 6, 7));
            u32x4 ov;
#pragma unroll
            for (int j = 0; j < 4; ++j) {
                const float u0 = __uint_as_float(uv[e][j] << 16), u1 = __uint_as_float(uv[e][j] & 0xffff0000u);
                const float d0f = __uint_as_float(dv[j] << 16), d1f = __uint_as_float(dv[j] & 0xffff0000u);
                const float y0f = __uint_as_float(yv[j] << 16), y1f = __uint_as_float(yv[j] & 0xffff0000u);
                ov[j] = pkbf(y0f + rbf(u0 * d0f), y1f + rbf(u1 * d1f));
            }
            *(u32x4*)((char*)p.out + (size_t)(64 * tp + 16 * ((g >> 1) + 2 * e) + tk) * 2048 + cg * 128 + sw * 32 + (g & 1) * 16) = ov;
        }
    }
    BAR();
    if (sw == 0 && lane == 0) set_flag(p.ws, WS_F2 + blockIdx.x * 128, tag);
    {
        const unsigned f2o = WS_F2 + (((lane & 31) >> 2) + 8 * (8 * (lane & 3) + xcd)) * 128;
        const char* ob = p.ws + WS_O + (size_t)tp * 65536;
        pend = 0x11111111u << sw;
#pragma nounroll
        for (unsigned spins = 0; spins < (1u << 16) && pend; ++spins) {
            unsigned rdy = (unsigned)__ballot(__hip_atomic_load((const gu64*)(p.ws + f2o), __ATOMIC_RELAXED, __HIP_MEMORY_SCOPE_AGENT) == tag) & pend;
            pend &= ~rdy;
            while (rdy) {
                const int hd = __builtin_ctz(rdy); rdy &= rdy - 1;
                glds16_dev(lane * 16, ob + hd * 1024, lds0 + L_OT + hd * 1024); glds16_dev(lane * 16, ob + (32 + hd) * 1024, lds0 + L_OT + (32 + hd) * 1024);
            }
            if (pend) __builtin_amdgcn_s_sleep(1);
        }
    }
    WAIT_VM(0); BAR();
    if (blockIdx.x == 0 && sw == 0 && lane == 0) set_flag(p.ws, WS_EPOCH, tag);
}

__global__ __launch_bounds__(768, 3) void s4d_mha_kernel(Params p) {
    __shared__ __attribute__((aligned(1024))) char lds[LDS_BYTES];
    LAS char* L = (LAS char*)lds;
    const unsigned lds0 = (unsigned)(uintptr_t)L;
    const int tid = threadIdx.x, lane = tid & 63, w = __builtin_amdgcn_readfirstlane(tid >> 6);
    const int xcd = blockIdx.x & 7, idx = blockIdx.x >> 3;
    unsigned long long pc = 0; unsigned len = 0;
    asm volatile("s_getpc_b64 %0\nL_pf_here:\n\ts_mov_b32 %1, L_pf_end1-L_pf_here\n\ts_max_i32 %1, %1, L_pf_end2-L_pf_here\n\ts_max_i32 %1, %1, 0" : "=s"(pc), "=s"(len) :: "scc");
    if (w < 2) {
        const unsigned off = (((idx & 3) * 2 + w) * 40 + lane) * 128;
        if (lane < 40) touch(off < len ? off : 0, (const char*)pc, lds0);
    }
    if (tid < 16) *(LAS unsigned*)(L + L_CNT + tid * 4) = 0;
    if (__builtin_expect(w >= 8, 1)) {
        service(p, L, lds0, w - 8, lane, xcd, idx);
        asm volatile("L_pf_end1:");
    } else {
        if (w < 4) phase_qkv(p, L, lds0, w, lane, xcd, idx);
        else phase_gen(p, L, w - 4, lane, xcd, idx);
        BAR();
        if (w < 4) {
            WAIT_VM(0);
            if (cnt_add(L, L_C1, lane) == 3 && lane == 0) set_flag(p.ws, WS_F1 + blockIdx.x * 128, *(const LAS unsigned long long*)(L + L_EP) + 1ull);
        }
        f32x16 y0, y1;
        conv_mfma(lds0, w, lane, y0, y1);
        if (w == 0 && lane == 0) *(volatile LAS unsigned*)(L + L_CB) = 1;
        BAR();
        conv_partials(L, w, lane, y0, y1);
        BAR();
        phase_att(p, L, lds0, w, lane, xcd, idx);
        WAIT_VM(0); BAR();
        oproj_stage_w(p, lds0, w, lane, idx);
        WAIT_VM(0);
        BAR();
        const f32x16 acc = oproj_mfma(lds0, w, lane);
        if (w >= 4) oproj_partial_out(L, w, lane, acc);
        BAR();
        if (w < 4) oproj_final(p, L, w, lane, xcd, idx, acc);
        asm volatile("L_pf_end2:");
    }
}

extern "C" void kernel_launch(void* const* d_in, const int* in_sizes, int n_in, void* d_out, int out_size, void* d_ws, size_t ws_size,
                              hipStream_t stream) {
    Params p;
    p.u = (const unsigned short*)d_in[0]; p.lar = (const unsigned short*)d_in[1]; p.aim = (const unsigned short*)d_in[2];
    p.cre = (const unsigned short*)d_in[3]; p.cim = (const unsigned short*)d_in[4]; p.ldt = (const unsigned short*)d_in[5];
    p.dsk = (const unsigned short*)d_in[6]; p.x = (const unsigned short*)d_in[7]; p.wqkv = (const unsigned short*)d_in[8];
    p.wo = (const unsigned short*)d_in[9]; p.out = (unsigned short*)d_out; p.ws = (char*)d_ws;
    s4d_mha_kernel<<<256, 768, 0, stream>>>(p);
}
```
